```python
import torch

def _softmax(x, axis=-1):
    return torch.softmax(x, dim=axis)
def _gelu(x):
    return torch.nn.functional.gelu(x, approximate="tanh")
def _silu(x):
    return torch.nn.functional.silu(x)
def _sigmoid(x):
    return torch.sigmoid(x)
def _layernorm(x, axis=-1, eps=1e-5):
    m = x.mean(dim=axis, keepdim=True)
    v = x.var(dim=axis, keepdim=True, unbiased=False)
    return (x - m) / torch.sqrt(v + eps)
def _scatter_add(tgt, idx, src, axis=0):
    return tgt.clone().index_add_(axis, idx, src)
def _segment_sum(src, seg, n):
    out = torch.zeros((n,) + src.shape[1:], dtype=src.dtype)
    return out.index_add_(0, seg, src)
def _pad(x, pw):
    flat = tuple(v for pair in reversed(pw) for v in pair)
    return torch.nn.functional.pad(x, flat)

kernel_name = 'standalone__rezero__sampling'

def setup_inputs():

    _w = (lambda a: (lambda t: t.to(torch.bfloat16) if t.is_floating_point() else t)(torch.as_tensor(a)))
    B, S, D = 1024, 32, 576
    return {
        "x": _w((lambda *s: torch.randn(*s, dtype=torch.bfloat16) * 0.05)(B, S, D)),
        "fx": _w((lambda *s: torch.randn(*s, dtype=torch.bfloat16) * 0.8)(B, S, D)),
        "alpha": _w((lambda *s: torch.randn(*s, dtype=torch.bfloat16) * 0.8)(1) * 0.1),
    }

def reference(x, fx, alpha):
    return x + alpha * fx
```

```cpp
#include <hip/hip_runtime.h>
#include <hip/hip_bf16.h>
#include <stdint.h>

#define P_NSTATIC 1536
#define P_SDELAY 7
#define P_SXCC 2
#define P_ORDER 1
#define P_RULE 0
#define P_MAXR 4
#define P_LDS 24576
#define P_LD 2
#define P_ST 17

typedef unsigned long long u64;
typedef unsigned int u32x4 __attribute__((ext_vector_type(4)));
typedef float f32x2 __attribute__((ext_vector_type(2)));
typedef __bf16 bf16x2 __attribute__((ext_vector_type(2)));
typedef __attribute__((address_space(1))) u64 gu64;
typedef const __attribute__((address_space(4))) unsigned int* cu32p;
typedef const __attribute__((address_space(4))) u64* cu64p;

constexpr unsigned int kBlock = 256;
constexpr unsigned int kScouts = 32;
constexpr unsigned int kRep = 32;

extern "C" __device__ u64 rz_dispatch_id() __asm__("llvm.amdgcn.dispatch.id");

__device__ __forceinline__ unsigned int pack_bf16x2(float lo, float hi) {
    f32x2 v = {lo, hi};
    return __builtin_bit_cast(unsigned int, __builtin_convertvector(v, bf16x2));
}
__device__ __forceinline__ unsigned int rezero2(unsigned int x, unsigned int f, float a) {
    const unsigned int t = pack_bf16x2(a * __builtin_bit_cast(float, f << 16), a * __builtin_bit_cast(float, f & 0xffff0000u));
    return pack_bf16x2(__builtin_bit_cast(float, x << 16) + __builtin_bit_cast(float, t << 16),
                       __builtin_bit_cast(float, x & 0xffff0000u) + __builtin_bit_cast(float, t & 0xffff0000u));
}
__device__ __forceinline__ u32x4 rezero8(u32x4 x, u32x4 f, float a) {
    u32x4 o;
    o.x = rezero2(x.x, f.x, a); o.y = rezero2(x.y, f.y, a); o.z = rezero2(x.z, f.z, a); o.w = rezero2(x.w, f.w, a);
    return o;
}
__device__ __forceinline__ float load_alpha(const unsigned short* alpha) {
    const uintptr_t p = (uintptr_t)alpha;
    const unsigned int w = *(cu32p)(p & ~(uintptr_t)3);
    return __builtin_bit_cast(float, (p & 2) ? (w & 0xffff0000u) : (w << 16));
}
__device__ __forceinline__ unsigned int timed_load(const char* a) {
    u64 t0, t1; unsigned int w;
    asm volatile("s_memtime %0\n\ts_waitcnt lgkmcnt(0)\n\ts_load_dword %1, %3, 0x0\n\ts_waitcnt lgkmcnt(0)\n\ts_memtime %2\n\ts_waitcnt lgkmcnt(0)"
                 : "=&s"(t0), "=&s"(w), "=&s"(t1) : "s"(a) : "memory");
    return (unsigned int)(t1 - t0);
}
#define RSRC(p, n) __builtin_amdgcn_make_buffer_rsrc((void*)(p), 0, (n), 0x00020000)

__global__ __launch_bounds__(kBlock) void rezero_kernel(const void* __restrict__ x, const void* __restrict__ fx,
                                                        const unsigned short* __restrict__ alpha, void* __restrict__ out,
                                                        u64* __restrict__ ws, unsigned int nbytes) {
    const unsigned int b0 = blockIdx.x;
    const unsigned int npairs = nbytes >> 14;
    const unsigned int lane = threadIdx.x & 63;
    const u64 did = rz_dispatch_id();
    const unsigned int tagH = (unsigned int)(did >> 8) ^ 0x5AC3E1F7u, tagL = ((unsigned int)did << 24) | 0x00A5D200u;

    if (__builtin_expect(b0 < kScouts, 0)) {
        __shared__ unsigned int s_lat[P_LDS / 4];
        const unsigned int w = b0 >> 3;
        if (w == 3 || npairs < P_NSTATIC / 2 + 256) return;
        const unsigned int wave = __builtin_amdgcn_readfirstlane(threadIdx.x >> 6);
        gu64* my = (gu64*)ws + (w * kRep + lane) * 8;
        const unsigned int pg = b0 & 1;
        const char* p = (const char*)(w == 0 ? x : (w == 1 ? fx : out)) + (pg << 21) + (wave & 1) * 8192;
        unsigned int h = ((unsigned int)did * 0x9E3779B1u + (b0 * 4 + wave) * 0x85EBCA6Bu) >> 9;
        unsigned int lat = ~0u;
#pragma unroll 1
        for (int k = 0; k < P_MAXR; ++k) {
            u64 old = 0;
            if (wave == 0 && lane < kRep) old = __hip_atomic_load(my, __ATOMIC_RELAXED, __HIP_MEMORY_SCOPE_AGENT);
            const unsigned int l = timed_load(p + (h & 63) * 16384 + ((h >> 6) & 31) * 256);
            h = (h >> 11) | (h << 21);
            lat = (l >= 380) ? min(lat, l) : lat;
            const bool set = ((unsigned int)(old >> 32) == tagH) && (((unsigned int)old ^ tagL) < 256);
            if (wave == 0) { const bool all = __all(set || lane >= kRep); if (lane == 0) s_lat[4] = all; }
            if (lane == 0) s_lat[wave] = lat;
            __syncthreads();
            const unsigned int la = __builtin_amdgcn_readfirstlane(min(s_lat[0], s_lat[2]));
            const unsigned int lb = __builtin_amdgcn_readfirstlane(min(s_lat[1], s_lat[3]));
            if (__builtin_amdgcn_readfirstlane(s_lat[4])) break;
            const unsigned int lo = min(la, lb), d = max(la, lb) - lo;
            const bool sure = d < 0x10000000u && ((lo < 800 && d >= 80) || (P_RULE == 0 && d >= 120));
            if (sure || k == P_MAXR - 1) {
                if (wave == 0 && lane < kRep && !set) {
                    unsigned int xcc; asm volatile("s_getreg_b32 %0, hwreg(HW_REG_XCC_ID, 0, 3)" : "=s"(xcc));
                    const unsigned int m = ((xcc >> 2) ^ (la >= lb) ^ pg) & 1;
                    const u64 want = ((u64)tagH << 32) | tagL | (((xcc - b0) & 7) << 1) | m;
                    __hip_atomic_compare_exchange_strong(my, &old, want, __ATOMIC_RELAXED, __ATOMIC_RELAXED, __HIP_MEMORY_SCOPE_AGENT);
                }
                break;
            }
            __syncthreads();
        }
        return;
    }

    const unsigned int b = b0 - kScouts;
    const unsigned int j = (b >> 3) * 4 + (b & 3);
    unsigned int t;
    if (__builtin_expect(j >= npairs, 0)) {
        const unsigned int npad = (npairs + 3) / 4 * 8;
        if (b < npad) return;
        t = 2 * npairs + (b - npad);
        if ((nbytes & 2) && b == npad && threadIdx.x == 0) {
            const unsigned int i = nbytes / 2 - 1;
            const unsigned int v = rezero2(((const unsigned short*)x)[i], ((const unsigned short*)fx)[i], load_alpha(alpha));
            ((unsigned short*)out)[i] = (unsigned short)v;
        }
    } else if (b < P_NSTATIC || npairs < P_NSTATIC / 2 + 256) {
        if (P_SDELAY) {
            unsigned int xcc; asm volatile("s_getreg_b32 %0, hwreg(HW_REG_XCC_ID, 0, 3)" : "=s"(xcc));
            if (xcc < P_SXCC) for (int i = 0; i < P_SDELAY; ++i) __builtin_amdgcn_s_sleep(4);
        }
        t = 2 * (npairs - 1 - j) + ((b >> 2) & 1);
        if (P_ORDER == 1) t = 2 * (npairs >= P_NSTATIC / 2 + 256 ? j + npairs - P_NSTATIC / 2 : j) + ((b >> 2) & 1);
    } else {
        const unsigned int q = j - P_NSTATIC / 2;
        const char* wl = (const char*)ws + (((b >> 3) + (b >> 8)) & (kRep - 1)) * 64;
        const u64 v0 = *(cu64p)(uintptr_t)wl, v1 = *(cu64p)(uintptr_t)(wl + kRep * 64), v2 = *(cu64p)(uintptr_t)(wl + 2 * kRep * 64);
        unsigned int d0 = (unsigned int)v0, d1 = (unsigned int)v1, d2 = (unsigned int)v2;
        if (__builtin_expect(!((unsigned int)(v0 >> 32) == tagH && (unsigned int)(v1 >> 32) == tagH && (unsigned int)(v2 >> 32) == tagH &&
                               ((d0 ^ tagL) | (d1 ^ tagL) | (d2 ^ tagL)) < 256), 0)) {
            gu64* my = (gu64*)(uintptr_t)(wl + (lane < 3 ? lane : 0) * kRep * 64);
            for (unsigned int spins = 0;; ++spins) {
                u64 v = __hip_atomic_load(my, __ATOMIC_RELAXED, __HIP_MEMORY_SCOPE_AGENT);
                const bool set = ((unsigned int)(v >> 32) == tagH) && (((unsigned int)v ^ tagL) < 256);
                if (__all(set)) {
                    d0 = __builtin_amdgcn_readlane((unsigned int)v, 0); d1 = __builtin_amdgcn_readlane((unsigned int)v, 1);
                    d2 = __builtin_amdgcn_readlane((unsigned int)v, 2);
                    break;
                }
                if (spins >= 32 && lane < 3 && !set)
                    __hip_atomic_compare_exchange_strong(my, &v, ((u64)tagH << 32) | tagL, __ATOMIC_RELAXED, __ATOMIC_RELAXED, __HIP_MEMORY_SCOPE_AGENT);
                __builtin_amdgcn_s_sleep(4);
            }
        }
        const unsigned int m = (d0 & d1) | (d0 & d2) | (d1 & d2);
        t = 2 * q + ((((b + (d0 >> 1)) >> 2) ^ m ^ (q >> 7)) & 1);
    }

    const __amdgpu_buffer_rsrc_t rx = RSRC(x, nbytes), rf = RSRC(fx, nbytes), ro = RSRC(out, nbytes);
    const unsigned int off = t * 8192 + threadIdx.x * 16;
    const u32x4 x0 = __builtin_amdgcn_raw_buffer_load_b128(rx, off, 0, P_LD);
    const u32x4 f0 = __builtin_amdgcn_raw_buffer_load_b128(rf, off, 0, P_LD);
    const u32x4 x1 = __builtin_amdgcn_raw_buffer_load_b128(rx, off + 4096, 0, P_LD);
    const u32x4 f1 = __builtin_amdgcn_raw_buffer_load_b128(rf, off + 4096, 0, P_LD);
    __builtin_amdgcn_sched_barrier(0);
    const float a = load_alpha(alpha);
    __builtin_amdgcn_raw_buffer_store_b128(rezero8(x0, f0, a), ro, off, 0, P_ST);
    __builtin_amdgcn_raw_buffer_store_b128(rezero8(x1, f1, a), ro, off + 4096, 0, P_ST);
}

extern "C" void kernel_launch(void* const* d_in, const int* in_sizes, int n_in,
                              void* d_out, int out_size, void* d_ws, size_t ws_size,
                              hipStream_t stream) {
    const unsigned int nbytes = (unsigned int)out_size * 2u;
    const unsigned int npairs = nbytes >> 14;
    const unsigned int npad = (npairs + 3) / 4 * 8;
    const unsigned int nrem = ((nbytes & 16383u) + 8191u) / 8192u;
    rezero_kernel<<<kScouts + npad + nrem, kBlock, 0, stream>>>(d_in[0], d_in[1], (const unsigned short*)d_in[2], d_out,
                                                               (u64*)d_ws, nbytes);
}
```

```cpp
#include <hip/hip_runtime.h>
#include <hip/hip_bf16.h>
#include <stdint.h>

#define P_NSTATIC 1536
#define P_SDELAY 7
#define P_SXCC 2
#define P_RULE 2
#define P_MG 60
#define P_PF -1
#define P_MAXR 4
#define P_LDS 24576
#define P_LD 2
#define P_ST 17

typedef unsigned long long u64;
typedef unsigned int u32x4 __attribute__((ext_vector_type(4)));
typedef float f32x2 __attribute__((ext_vector_type(2)));
typedef __bf16 bf16x2 __attribute__((ext_vector_type(2)));
typedef __attribute__((address_space(1))) u64 gu64;
typedef const __attribute__((address_space(4))) unsigned int* cu32p;
typedef const __attribute__((address_space(4))) u64* cu64p;

constexpr unsigned int kBlock = 256;
constexpr unsigned int kScouts = 32;
constexpr unsigned int kRep = 32;

extern "C" __device__ u64 rz_dispatch_id() __asm__("llvm.amdgcn.dispatch.id");

__device__ __forceinline__ unsigned int pack_bf16x2(float lo, float hi) {
    f32x2 v = {lo, hi};
    return __builtin_bit_cast(unsigned int, __builtin_convertvector(v, bf16x2));
}
__device__ __forceinline__ unsigned int rezero2(unsigned int x, unsigned int f, float a) {
    const unsigned int t = pack_bf16x2(a * __builtin_bit_cast(float, f << 16), a * __builtin_bit_cast(float, f & 0xffff0000u));
    return pack_bf16x2(__builtin_bit_cast(float, x << 16) + __builtin_bit_cast(float, t << 16),
                       __builtin_bit_cast(float, x & 0xffff0000u) + __builtin_bit_cast(float, t & 0xffff0000u));
}
__device__ __forceinline__ u32x4 rezero8(u32x4 x, u32x4 f, float a) {
    u32x4 o;
    o.x = rezero2(x.x, f.x, a); o.y = rezero2(x.y, f.y, a); o.z = rezero2(x.z, f.z, a); o.w = rezero2(x.w, f.w, a);
    return o;
}
__device__ __forceinline__ float load_alpha(const unsigned short* alpha) {
    const uintptr_t p = (uintptr_t)alpha;
    const unsigned int w = *(cu32p)(p & ~(uintptr_t)3);
    return __builtin_bit_cast(float, (p & 2) ? (w & 0xffff0000u) : (w << 16));
}
__device__ __forceinline__ unsigned int timed_load(const char* a) {
    u64 t0, t1; unsigned int w;
    asm volatile("s_memtime %0\n\ts_waitcnt lgkmcnt(0)\n\ts_load_dword %1, %3, 0x0\n\ts_waitcnt lgkmcnt(0)\n\ts_memtime %2\n\ts_waitcnt lgkmcnt(0)"
                 : "=&s"(t0), "=&s"(w), "=&s"(t1) : "s"(a) : "memory");
    return (unsigned int)(t1 - t0);
}
#define RSRC(p, n) __builtin_amdgcn_make_buffer_rsrc((void*)(p), 0, (n), 0x00020000)

__global__ __launch_bounds__(kBlock) void rezero_kernel(const void* __restrict__ x, const void* __restrict__ fx,
                                                        const unsigned short* __restrict__ alpha, void* __restrict__ out,
                                                        u64* __restrict__ ws, unsigned int nbytes, unsigned int nfixed, unsigned int qfixed) {
    const unsigned int b0 = blockIdx.x;
    const unsigned int b = b0 - kScouts;
    const unsigned int j = (b >> 3) * 4 + (b & 3);
    unsigned int t;
    if (__builtin_expect(b < nfixed, 1)) {
        if (P_SDELAY) {
            unsigned int xcc; asm volatile("s_getreg_b32 %0, hwreg(HW_REG_XCC_ID, 0, 3)" : "=s"(xcc));
            if (xcc < P_SXCC) __builtin_amdgcn_s_sleep(P_SDELAY * 4);
        }
        t = 2 * (j + qfixed) + ((b >> 2) & 1);
    } else {
        const unsigned int npairs = nbytes >> 14;
        const unsigned int lane = threadIdx.x & 63;
        const u64 did = rz_dispatch_id();
        const unsigned int tagH = (unsigned int)(did >> 8) ^ 0x5AC3E1F7u, tagL = ((unsigned int)did << 24) | 0x00A5D200u;
        if (__builtin_expect(b0 < kScouts, 0)) {
        __shared__ unsigned int s_lat[P_LDS / 4];
        const unsigned int w = b0 >> 3;
        if (nfixed == 0) return;
        if (w == 3) {
            if (P_PF >= 0) {
                for (int i = 0; i < P_PF; ++i) __builtin_amdgcn_s_sleep(4);
                const unsigned int arr = threadIdx.x % 3, page = threadIdx.x / 3;
                const unsigned int v = __builtin_amdgcn_raw_buffer_load_b32(RSRC(x, nbytes), arr == 0 ? page << 21 : ~0u, 0, 2) +
                                       __builtin_amdgcn_raw_buffer_load_b32(RSRC(fx, nbytes), arr == 1 ? page << 21 : ~0u, 0, 2) +
                                       __builtin_amdgcn_raw_buffer_load_b32(RSRC(out, nbytes), arr == 2 ? page << 21 : ~0u, 0, 2);
                asm volatile("" :: "v"(v));
            }
            return;
        }
        const unsigned int wave = __builtin_amdgcn_readfirstlane(threadIdx.x >> 6);
        gu64* my = (gu64*)ws + (w * kRep + lane) * 8;
        const unsigned int pg = b0 & 1;
        const char* p = (const char*)(w == 0 ? x : (w == 1 ? fx : out)) + (pg << 21) + (wave & 1) * 8192;
        unsigned int h = ((unsigned int)did * 0x9E3779B1u + (b0 * 4 + wave) * 0x85EBCA6Bu) >> 9;
        unsigned int lat = ~0u;
#pragma unroll 1
        for (int k = 0; k < P_MAXR; ++k) {
            u64 old = 0;
            if (wave == 0 && lane < kRep) old = __hip_atomic_load(my, __ATOMIC_RELAXED, __HIP_MEMORY_SCOPE_AGENT);
            const unsigned int l = timed_load(p + (h & 63) * 16384 + ((h >> 6) & 31) * 256);
            h = (h >> 11) | (h << 21);
            lat = (l >= 380) ? min(lat, l) : lat;
            const bool set = ((unsigned int)(old >> 32) == tagH) && (((unsigned int)old ^ tagL) < 256);
            if (wave == 0) { const bool all = __all(set || lane >= kRep); if (lane == 0) s_lat[4] = all; }
            if (lane == 0) s_lat[wave] = lat;
            __syncthreads();
            const unsigned int l0 = __builtin_amdgcn_readfirstlane(s_lat[0]), l1 = __builtin_amdgcn_readfirstlane(s_lat[1]);
            const unsigned int l2 = __builtin_amdgcn_readfirstlane(s_lat[2]), l3 = __builtin_amdgcn_readfirstlane(s_lat[3]);
            if (__builtin_amdgcn_readfirstlane(s_lat[4])) break;
            const unsigned int la = min(l0, l2), lb = min(l1, l3);
            const unsigned int lo = min(la, lb), d = max(la, lb) - lo;
            bool sure = d < 0x10000000u && lo < 800 && d >= 80;
            if (P_RULE == 0) sure = sure || (d >= 120 && d < 0x10000000u);
            if (P_RULE == 2) sure = sure || (k == 0 && d >= P_MG && max(max(l0, l1), max(l2, l3)) != ~0u && (l0 < l1) == (l2 < l3) && (l0 < l1) == (la < lb));
            if (sure || k == P_MAXR - 1) {
                if (wave == 0 && lane < kRep && !set) {
                    unsigned int xcc; asm volatile("s_getreg_b32 %0, hwreg(HW_REG_XCC_ID, 0, 3)" : "=s"(xcc));
                    const unsigned int m = ((xcc >> 2) ^ (la >= lb) ^ pg) & 1;
                    const u64 want = ((u64)tagH << 32) | tagL | (((xcc - b0) & 7) << 1) | m;
                    __hip_atomic_compare_exchange_strong(my, &old, want, __ATOMIC_RELAXED, __ATOMIC_RELAXED, __HIP_MEMORY_SCOPE_AGENT);
                }
                break;
            }
            __syncthreads();
        }
            return;
        }
        if (__builtin_expect(j >= npairs || nfixed == 0, 0)) {
            const unsigned int npad = (npairs + 3) / 4 * 8;
            if (j < npairs && b < npad) {
                t = 2 * j + ((b >> 2) & 1);
            } else {
                if (b < npad) return;
                t = 2 * npairs + (b - npad);
                if ((nbytes & 2) && b == npad && threadIdx.x == 0) {
                    const unsigned int i = nbytes / 2 - 1;
                    const unsigned int v = rezero2(((const unsigned short*)x)[i], ((const unsigned short*)fx)[i], load_alpha(alpha));
                    ((unsigned short*)out)[i] = (unsigned short)v;
                }
            }
        } else {
            const unsigned int q = j - nfixed / 2;
        const char* wl = (const char*)ws + (((b >> 3) + (b >> 8)) & (kRep - 1)) * 64;
        const u64 v0 = *(cu64p)(uintptr_t)wl, v1 = *(cu64p)(uintptr_t)(wl + kRep * 64), v2 = *(cu64p)(uintptr_t)(wl + 2 * kRep * 64);
        unsigned int d0 = (unsigned int)v0, d1 = (unsigned int)v1, d2 = (unsigned int)v2;
        if (__builtin_expect(!((unsigned int)(v0 >> 32) == tagH && (unsigned int)(v1 >> 32) == tagH && (unsigned int)(v2 >> 32) == tagH &&
                               ((d0 ^ tagL) | (d1 ^ tagL) | (d2 ^ tagL)) < 256), 0)) {
            gu64* my = (gu64*)(uintptr_t)(wl + (lane < 3 ? lane : 0) * kRep * 64);
            for (unsigned int spins = 0;; ++spins) {
                u64 v = __hip_atomic_load(my, __ATOMIC_RELAXED, __HIP_MEMORY_SCOPE_AGENT);
                const bool set = ((unsigned int)(v >> 32) == tagH) && (((unsigned int)v ^ tagL) < 256);
                if (__all(set)) {
                    d0 = __builtin_amdgcn_readlane((unsigned int)v, 0); d1 = __builtin_amdgcn_readlane((unsigned int)v, 1);
                    d2 = __builtin_amdgcn_readlane((unsigned int)v, 2);
                    break;
                }
                if (spins >= 32 && lane < 3 && !set)
                    __hip_atomic_compare_exchange_strong(my, &v, ((u64)tagH << 32) | tagL, __ATOMIC_RELAXED, __ATOMIC_RELAXED, __HIP_MEMORY_SCOPE_AGENT);
                __builtin_amdgcn_s_sleep(4);
            }
        }
            const unsigned int m = (d0 & d1) | (d0 & d2) | (d1 & d2);
            t = 2 * q + ((((b + (d0 >> 1)) >> 2) ^ m ^ (q >> 7)) & 1);
        }
    }

    const __amdgpu_buffer_rsrc_t rx = RSRC(x, nbytes), rf = RSRC(fx, nbytes), ro = RSRC(out, nbytes);
    const unsigned int off = t * 8192 + threadIdx.x * 16;
    const u32x4 x0 = __builtin_amdgcn_raw_buffer_load_b128(rx, off, 0, P_LD);
    const u32x4 f0 = __builtin_amdgcn_raw_buffer_load_b128(rf, off, 0, P_LD);
    const u32x4 x1 = __builtin_amdgcn_raw_buffer_load_b128(rx, off + 4096, 0, P_LD);
    const u32x4 f1 = __builtin_amdgcn_raw_buffer_load_b128(rf, off + 4096, 0, P_LD);
    __builtin_amdgcn_sched_barrier(0);
    const float a = load_alpha(alpha);
    __builtin_amdgcn_raw_buffer_store_b128(rezero8(x0, f0, a), ro, off, 0, P_ST);
    __builtin_amdgcn_raw_buffer_store_b128(rezero8(x1, f1, a), ro, off + 4096, 0, P_ST);
}

extern "C" void kernel_launch(void* const* d_in, const int* in_sizes, int n_in,
                              void* d_out, int out_size, void* d_ws, size_t ws_size,
                              hipStream_t stream) {
    const unsigned int nbytes = (unsigned int)out_size * 2u;
    const unsigned int npairs = nbytes >> 14;
    const unsigned int npad = (npairs + 3) / 4 * 8;
    const unsigned int nrem = ((nbytes & 16383u) + 8191u) / 8192u;
    const bool big = npairs >= P_NSTATIC / 2 + 256;
    const unsigned int nfixed = big ? P_NSTATIC : 0u, qfixed = big ? npairs - P_NSTATIC / 2 : 0u;
    rezero_kernel<<<kScouts + npad + nrem, kBlock, 0, stream>>>(d_in[0], d_in[1], (const unsigned short*)d_in[2], d_out,
                                                               (u64*)d_ws, nbytes, nfixed, qfixed);
}
```

```cpp
#include <hip/hip_runtime.h>
#include <hip/hip_bf16.h>
#include <stdint.h>

typedef unsigned long long u64;
typedef unsigned int u32x4 __attribute__((ext_vector_type(4)));
typedef float f32x2 __attribute__((ext_vector_type(2)));
typedef __bf16 bf16x2 __attribute__((ext_vector_type(2)));
typedef __attribute__((address_space(1))) u64 gu64;
typedef const __attribute__((address_space(4))) unsigned int* cu32p;
typedef const __attribute__((address_space(4))) u64* cu64p;

constexpr unsigned int kBlock = 256;
constexpr unsigned int kLdsBytes = 24576;
constexpr unsigned int kFixed = 1536;
constexpr unsigned int kScouts = 32;
constexpr unsigned int kRep = 32;
constexpr int kRounds = 4;
constexpr int kLoadAux = 2;
constexpr int kStoreAux = 17;

extern "C" __device__ u64 rz_dispatch_id() __asm__("llvm.amdgcn.dispatch.id");

__device__ __forceinline__ unsigned int pack_bf16x2(float lo, float hi) {
    const f32x2 v = {lo, hi};
    return __builtin_bit_cast(unsigned int, __builtin_convertvector(v, bf16x2));
}
__device__ __forceinline__ unsigned int rezero2(unsigned int x, unsigned int f, float a) {
    const unsigned int t = pack_bf16x2(a * __builtin_bit_cast(float, f << 16), a * __builtin_bit_cast(float, f & 0xffff0000u));
    return pack_bf16x2(__builtin_bit_cast(float, x << 16) + __builtin_bit_cast(float, t << 16),
                       __builtin_bit_cast(float, x & 0xffff0000u) + __builtin_bit_cast(float, t & 0xffff0000u));
}
__device__ __forceinline__ u32x4 rezero8(u32x4 x, u32x4 f, float a) {
    u32x4 o;
    o.x = rezero2(x.x, f.x, a); o.y = rezero2(x.y, f.y, a); o.z = rezero2(x.z, f.z, a); o.w = rezero2(x.w, f.w, a);
    return o;
}
__device__ __forceinline__ float load_alpha(const unsigned short* alpha) {
    const uintptr_t p = (uintptr_t)alpha;
    const unsigned int w = *(cu32p)(p & ~(uintptr_t)3);
    return __builtin_bit_cast(float, (p & 2) ? (w & 0xffff0000u) : (w << 16));
}
__device__ __forceinline__ unsigned int xcc_id() {
    unsigned int v;
    asm volatile("s_getreg_b32 %0, hwreg(HW_REG_XCC_ID, 0, 3)" : "=s"(v));
    return v;
}
__device__ __forceinline__ unsigned int timed_load(const char* a) {
    u64 t0, t1;
    unsigned int w;
    asm volatile("s_memtime %0\n\ts_waitcnt lgkmcnt(0)\n\ts_load_dword %1, %3, 0x0\n\ts_waitcnt lgkmcnt(0)\n\ts_memtime %2\n\ts_waitcnt lgkmcnt(0)"
                 : "=&s"(t0), "=&s"(w), "=&s"(t1) : "s"(a) : "memory");
    return (unsigned int)(t1 - t0);
}
#define RZ_RSRC(p, n) __builtin_amdgcn_make_buffer_rsrc((void*)(p), 0, (n), 0x00020000)

__global__ __launch_bounds__(kBlock) void rezero_kernel(const void* __restrict__ x, const void* __restrict__ fx,
                                                        void* __restrict__ out, const unsigned short* __restrict__ alpha,
                                                        unsigned int nbytes, unsigned int nfixed, unsigned int qfixed,
                                                        unsigned int npairs, u64* __restrict__ ws) {
    const unsigned int b0 = blockIdx.x;
    const unsigned int b = b0 - kScouts;
    const unsigned int j = (b >> 3) * 4 + (b & 3);
    unsigned int t;
    if (__builtin_expect(b < nfixed, 1)) {
        if (xcc_id() < 2) __builtin_amdgcn_s_sleep(30);
        t = 2 * (j + qfixed) + ((b >> 2) & 1);
    } else {
        const unsigned int lane = threadIdx.x & 63;
        const u64 did = rz_dispatch_id();
        const unsigned int tagH = (unsigned int)(did >> 8) ^ 0x5AC3E1F7u, tagL = ((unsigned int)did << 24) | 0x00A5D200u;
        if (__builtin_expect(b0 < kScouts, 0)) {
            __shared__ unsigned int s_lat[kLdsBytes / 4];
            const unsigned int w = b0 >> 3;
            if (w == 3 || nfixed == 0) return;
            const unsigned int wave = __builtin_amdgcn_readfirstlane(threadIdx.x >> 6);
            gu64* my = (gu64*)ws + (w * kRep + lane) * 8;
            const unsigned int pg = b0 & 1;
            const char* p = (const char*)(w == 0 ? x : (w == 1 ? fx : (const void*)out)) + (pg << 21) + (wave & 1) * 8192;
            unsigned int h = ((unsigned int)did * 0x9E3779B1u + (b0 * 4 + wave) * 0x85EBCA6Bu) >> 9;
            unsigned int lat = ~0u;
#pragma unroll 1
            for (int k = 0; k < kRounds; ++k) {
                u64 old = 0;
                if (wave == 0 && lane < kRep) old = __hip_atomic_load(my, __ATOMIC_RELAXED, __HIP_MEMORY_SCOPE_AGENT);
                const unsigned int l = timed_load(p + (h & 63) * 16384 + ((h >> 6) & 31) * 256);
                h = (h >> 11) | (h << 21);
                lat = (l >= 380) ? min(lat, l) : lat;
                const bool set = ((unsigned int)(old >> 32) == tagH) && (((unsigned int)old ^ tagL) < 256);
                if (wave == 0) { const bool all = __all(set || lane >= kRep); if (lane == 0) s_lat[4] = all; }
                if (lane == 0) s_lat[wave] = lat;
                __syncthreads();
                const unsigned int l0 = __builtin_amdgcn_readfirstlane(s_lat[0]), l1 = __builtin_amdgcn_readfirstlane(s_lat[1]);
                const unsigned int l2 = __builtin_amdgcn_readfirstlane(s_lat[2]), l3 = __builtin_amdgcn_readfirstlane(s_lat[3]);
                if (__builtin_amdgcn_readfirstlane(s_lat[4])) break;
                const unsigned int la = min(l0, l2), lb = min(l1, l3);
                const unsigned int lo = min(la, lb), d = max(la, lb) - lo;
                const bool sure = (lo < 800 && d >= 80 && d < 0x10000000u) ||
                                  (k == 0 && d >= 60 && max(max(l0, l1), max(l2, l3)) != ~0u && (l0 < l1) == (l2 < l3) && (l0 < l1) == (la < lb));
                if (sure || k == kRounds - 1) {
                    if (wave == 0 && lane < kRep && !set) {
                        const unsigned int xcc = xcc_id();
                        const unsigned int m = ((xcc >> 2) ^ (la >= lb) ^ pg) & 1;
                        const u64 want = ((u64)tagH << 32) | tagL | (((xcc - b0) & 7) << 1) | m;
                        __hip_atomic_compare_exchange_strong(my, &old, want, __ATOMIC_RELAXED, __ATOMIC_RELAXED, __HIP_MEMORY_SCOPE_AGENT);
                    }
                    break;
                }
                __syncthreads();
            }
            return;
        }
        if (__builtin_expect(j >= npairs || nfixed == 0, 0)) {
            const unsigned int npad = (npairs + 3) / 4 * 8;
            if (j < npairs && b < npad) {
                t = 2 * j + ((b >> 2) & 1);
            } else {
                if (b < npad) return;
                t = 2 * npairs + (b - npad);
                if ((nbytes & 2) && b == npad && threadIdx.x == 0) {
                    const unsigned int i = nbytes / 2 - 1;
                    const unsigned int v = rezero2(((const unsigned short*)x)[i], ((const unsigned short*)fx)[i], load_alpha(alpha));
                    ((unsigned short*)out)[i] = (unsigned short)v;
                }
            }
        } else {
            const unsigned int q = j - nfixed / 2;
            const char* wl = (const char*)ws + (((b >> 3) + (b >> 8)) & (kRep - 1)) * 64;
            const u64 v0 = *(cu64p)(uintptr_t)wl, v1 = *(cu64p)(uintptr_t)(wl + kRep * 64), v2 = *(cu64p)(uintptr_t)(wl + 2 * kRep * 64);
            unsigned int d0 = (unsigned int)v0, d1 = (unsigned int)v1, d2 = (unsigned int)v2;
            if (__builtin_expect(!((unsigned int)(v0 >> 32) == tagH && (unsigned int)(v1 >> 32) == tagH && (unsigned int)(v2 >> 32) == tagH &&
                                   ((d0 ^ tagL) | (d1 ^ tagL) | (d2 ^ tagL)) < 256), 0)) {
                gu64* my = (gu64*)(uintptr_t)(wl + (lane < 3 ? lane : 0) * kRep * 64);
                for (unsigned int spins = 0;; ++spins) {
                    u64 v = __hip_atomic_load(my, __ATOMIC_RELAXED, __HIP_MEMORY_SCOPE_AGENT);
                    const bool set = ((unsigned int)(v >> 32) == tagH) && (((unsigned int)v ^ tagL) < 256);
                    if (__all(set)) {
                        d0 = __builtin_amdgcn_readlane((unsigned int)v, 0);
                        d1 = __builtin_amdgcn_readlane((unsigned int)v, 1);
                        d2 = __builtin_amdgcn_readlane((unsigned int)v, 2);
                        break;
                    }
                    if (spins >= 32 && lane < 3 && !set)
                        __hip_atomic_compare_exchange_strong(my, &v, ((u64)tagH << 32) | tagL, __ATOMIC_RELAXED, __ATOMIC_RELAXED,
                                                             __HIP_MEMORY_SCOPE_AGENT);
                    __builtin_amdgcn_s_sleep(4);
                }
            }
            const unsigned int m = (d0 & d1) | (d0 & d2) | (d1 & d2);
            t = 2 * q + ((((b + (d0 >> 1)) >> 2) ^ m ^ (q >> 7)) & 1);
        }
    }

    const __amdgpu_buffer_rsrc_t rx = RZ_RSRC(x, nbytes), rf = RZ_RSRC(fx, nbytes), ro = RZ_RSRC(out, nbytes);
    const unsigned int off = t * 8192 + threadIdx.x * 16;
    const u32x4 x0 = __builtin_amdgcn_raw_buffer_load_b128(rx, off, 0, kLoadAux);
    const u32x4 f0 = __builtin_amdgcn_raw_buffer_load_b128(rf, off, 0, kLoadAux);
    const u32x4 x1 = __builtin_amdgcn_raw_buffer_load_b128(rx, off + 4096, 0, kLoadAux);
    const u32x4 f1 = __builtin_amdgcn_raw_buffer_load_b128(rf, off + 4096, 0, kLoadAux);
    __builtin_amdgcn_sched_barrier(0);
    const float a = load_alpha(alpha);
    __builtin_amdgcn_raw_buffer_store_b128(rezero8(x0, f0, a), ro, off, 0, kStoreAux);
    __builtin_amdgcn_raw_buffer_store_b128(rezero8(x1, f1, a), ro, off + 4096, 0, kStoreAux);
}

extern "C" void kernel_launch(void* const* d_in, const int* in_sizes, int n_in,
                              void* d_out, int out_size, void* d_ws, size_t ws_size,
                              hipStream_t stream) {
    const unsigned int nbytes = (unsigned int)out_size * 2u;
    const unsigned int npairs = nbytes >> 14;
    const unsigned int npad = (npairs + 3) / 4 * 8;
    const unsigned int nrem = ((nbytes & 16383u) + 8191u) / 8192u;
    const bool big = npairs >= kFixed / 2 + 256;
    const unsigned int nfixed = big ? kFixed : 0u, qfixed = big ? npairs - kFixed / 2 : 0u;
    rezero_kernel<<<kScouts + npad + nrem, kBlock, 0, stream>>>(d_in[0], d_in[1], d_out, (const unsigned short*)d_in[2], nbytes,
                                                                nfixed, qfixed, npairs, (u64*)d_ws);
}
```

```cpp
#include <hip/hip_runtime.h>
#include <hip/hip_bf16.h>
#include <stdint.h>

typedef unsigned long long u64;
typedef unsigned int u32x4 __attribute__((ext_vector_type(4)));
typedef unsigned int u32x16 __attribute__((ext_vector_type(16)));
typedef float f32x2 __attribute__((ext_vector_type(2)));
typedef __bf16 bf16x2 __attribute__((ext_vector_type(2)));
typedef __attribute__((address_space(1))) u64 gu64;
typedef const __attribute__((address_space(4))) unsigned int* cu32p;
typedef const __attribute__((address_space(4))) u64* cu64p;
typedef const __attribute__((address_space(4))) u32x16* cu32x16p;

constexpr unsigned int kBlock = 256;
constexpr unsigned int kLdsBytes = 24576;
constexpr unsigned int kFixed = 1536;
constexpr unsigned int kScouts = 32;
constexpr unsigned int kRep = 32;
constexpr int kRounds = 4;
constexpr int kLoadAux = 2;
constexpr int kHoldCu = 80;
constexpr int kStoreAux = 17;

__device__ u64 rz_words[(3 * kRep + 3 * kRounds) * 8];

extern "C" __device__ u64 rz_dispatch_id() __asm__("llvm.amdgcn.dispatch.id");

__device__ __forceinline__ unsigned int pack_bf16x2(float lo, float hi) {
    const f32x2 v = {lo, hi};
    return __builtin_bit_cast(unsigned int, __builtin_convertvector(v, bf16x2));
}
__device__ __forceinline__ unsigned int rezero2(unsigned int x, unsigned int f, float a) {
    const unsigned int t = pack_bf16x2(a * __builtin_bit_cast(float, f << 16), a * __builtin_bit_cast(float, f & 0xffff0000u));
    return pack_bf16x2(__builtin_bit_cast(float, x << 16) + __builtin_bit_cast(float, t << 16),
                       __builtin_bit_cast(float, x & 0xffff0000u) + __builtin_bit_cast(float, t & 0xffff0000u));
}
__device__ __forceinline__ u32x4 rezero8(u32x4 x, u32x4 f, float a) {
    u32x4 o;
    o.x = rezero2(x.x, f.x, a); o.y = rezero2(x.y, f.y, a); o.z = rezero2(x.z, f.z, a); o.w = rezero2(x.w, f.w, a);
    return o;
}
__device__ __forceinline__ float load_alpha(const unsigned short* alpha) {
    const uintptr_t p = (uintptr_t)alpha;
    const unsigned int w = *(cu32p)(p & ~(uintptr_t)3);
    return __builtin_bit_cast(float, (p & 2) ? (w & 0xffff0000u) : (w << 16));
}
__device__ __forceinline__ unsigned int xcc_id() {
    unsigned int v;
    asm volatile("s_getreg_b32 %0, hwreg(HW_REG_XCC_ID, 0, 3)" : "=s"(v));
    return v;
}
__device__ __forceinline__ void lds_barrier() {
    asm volatile("s_waitcnt lgkmcnt(0)\n\ts_barrier" ::: "memory");
}
__device__ __forceinline__ unsigned int timed_load(const char* a) {
    u64 t0, t1;
    unsigned int w;
    asm volatile("s_memtime %0\n\ts_waitcnt lgkmcnt(0)\n\ts_load_dword %1, %3, 0x0\n\ts_waitcnt lgkmcnt(0)\n\ts_memtime %2\n\ts_waitcnt lgkmcnt(0)"
                 : "=&s"(t0), "=&s"(w), "=&s"(t1) : "s"(a) : "memory");
    return (unsigned int)(t1 - t0);
}
#define RZ_RSRC(p, n) __builtin_amdgcn_make_buffer_rsrc((void*)(p), 0, (n), 0x00020000)

__global__ __launch_bounds__(kBlock) void rezero_kernel(const void* __restrict__ x, const void* __restrict__ fx,
                                                        void* __restrict__ out, const unsigned short* __restrict__ alpha,
                                                        unsigned int nbytes, unsigned int nfixed, unsigned int qfixed,
                                                        unsigned int npairs) {
    const unsigned int b0 = blockIdx.x;
    const unsigned int b = b0 - kScouts;
    const unsigned int j = (b >> 3) * 4 + (b & 3);
    unsigned int t;
    if (__builtin_expect(b < nfixed, 1)) {
        if (((b0 >> 3) & 31) < 3) {
            __builtin_amdgcn_s_sleep(kHoldCu);
        } else {
            const unsigned int wave = __builtin_amdgcn_readfirstlane(threadIdx.x >> 6);
            const unsigned int first = __builtin_amdgcn_raw_buffer_load_b32(RZ_RSRC(wave == 0 ? x : (wave == 1 ? fx : (const void*)out), nbytes),
                                                                            ((j + qfixed) >> 7) << 21, 0, kLoadAux);
            asm volatile("" :: "v"(first));
            lds_barrier();
        }
        t = 2 * (j + qfixed) + ((b >> 2) & 1);
    } else {
        const unsigned int lane = threadIdx.x & 63;
        const u64 did = rz_dispatch_id();
        const unsigned int tagH = (unsigned int)(did >> 8) ^ 0x5AC3E1F7u, tagL = ((unsigned int)did << 24) | 0x00A5D200u;
        if (__builtin_expect(b0 < kScouts, 0)) {
            __shared__ unsigned int s_lat[kLdsBytes / 4];
            const unsigned int w = b0 >> 3;
            if (w == 3 || nfixed == 0) return;
            const unsigned int wave = __builtin_amdgcn_readfirstlane(threadIdx.x >> 6);
            gu64* my = (gu64*)rz_words + (w * kRep + lane) * 8;
            const unsigned int pg = b0 & 1;
            const char* p = (const char*)(w == 0 ? x : (w == 1 ? fx : (const void*)out)) + (pg << 21) + (wave & 1) * 8192;
            unsigned int h = (unsigned int)did * 0x9E3779B1u + (b0 * 4 + wave) * 0x85EBCA6Bu;
            u64 old = 0;
            if (wave == 0 && lane < kRep) old = __hip_atomic_load(my, __ATOMIC_RELAXED, __HIP_MEMORY_SCOPE_AGENT);
            char* pool = (char*)rz_words + (3 * kRep + w * kRounds) * 64;
            const unsigned int xcc = xcc_id(), tagS = tagH ^ 0x3C5A96E7u;
            const bool evenNearIf0 = ((xcc >> 2) ^ pg) == 0;
            unsigned int lat = ~0u;
#pragma unroll
            for (int k = 0; k < kRounds; ++k) {
                const unsigned int l = timed_load(p + ((h >> 20) & 63) * 16384 + ((h >> 26) & 31) * 256);
                h = h * 0x2C1B3C6Du + 0x297A2D39u;
                lat = (l >= 380) ? min(lat, l) : lat;
                if (lane == 0) s_lat[wave] = lat;
                lds_barrier();
                const unsigned int l0 = __builtin_amdgcn_readfirstlane(s_lat[0]), l1 = __builtin_amdgcn_readfirstlane(s_lat[1]);
                const unsigned int l2 = __builtin_amdgcn_readfirstlane(s_lat[2]), l3 = __builtin_amdgcn_readfirstlane(s_lat[3]);
                const unsigned int la = min(min(l0, l2), 0xFFFFu), lb = min(min(l1, l3), 0xFFFFu);
                unsigned int u = evenNearIf0 ? la : lb, v = evenNearIf0 ? lb : la;
                if (threadIdx.x == 64)
                    __hip_atomic_store((gu64*)(uintptr_t)(pool + k * 64) + (b0 & 7), ((u64)tagS << 32) | (u << 16) | v, __ATOMIC_RELAXED,
                                       __HIP_MEMORY_SCOPE_AGENT);
                bool sure = min(u, v) < 800 && max(u, v) - min(u, v) >= 80 && max(u, v) != 0xFFFFu;
                if (k == 0) {
                    sure = sure || (max(u, v) - min(u, v) >= 60 && min(u, v) < 1500 && max(max(l0, l1), max(l2, l3)) != ~0u &&
                                    (l0 < l1) == (l2 < l3) && (l0 < l1) == (la < lb));
                } else if (!sure) {
                    const u32x16 o = *(cu32x16p)(uintptr_t)(pool + (k - 1) * 64);
                    unsigned int samples = 2 * k;
#pragma unroll
                    for (int i = 0; i < 8; ++i) {
                        const bool fresh = o[2 * i + 1] == tagS && i != (int)(b0 & 7);
                        u = fresh ? min(u, o[2 * i] >> 16) : u;
                        v = fresh ? min(v, o[2 * i] & 0xFFFFu) : v;
                        samples += (fresh && k > 1) ? 2 : 0;
                    }
                    sure = max(u, v) - min(u, v) >= (samples >= 6 ? 60u : 120u) && min(u, v) < 1100 && max(u, v) != 0xFFFFu;
                }
                if (sure || k == kRounds - 1) {
                    bool answered = false;
                    if (xcc >= 2 || k > 0) {
                        const u64 z = *(cu64p)(uintptr_t)((const char*)rz_words + (w * kRep + (b0 & 7) * 4 + k) * 64);
                        answered = (unsigned int)(z >> 32) == tagH && ((unsigned int)z ^ tagL) < 256;
                    }
                    u64 expected = old;
                    asm volatile("" : "+v"(expected));
                    const bool set = ((unsigned int)(expected >> 32) == tagH) && (((unsigned int)expected ^ tagL) < 256);
                    if (wave == 0 && lane < kRep && !set && !answered) {
                        const u64 want = ((u64)tagH << 32) | tagL | (((xcc - b0) & 7) << 1) | (u <= v ? 0u : 1u);
                        __hip_atomic_compare_exchange_strong(my, &expected, want, __ATOMIC_RELAXED, __ATOMIC_RELAXED, __HIP_MEMORY_SCOPE_AGENT);
                    }
                    break;
                }
                lds_barrier();
            }
            return;
        }
        if (__builtin_expect(j >= npairs || nfixed == 0, 0)) {
            const unsigned int npad = (npairs + 3) / 4 * 8;
            if (j < npairs && b < npad) {
                t = 2 * j + ((b >> 2) & 1);
            } else {
                if (b < npad) return;
                t = 2 * npairs + (b - npad);
                if ((nbytes & 2) && b == npad && threadIdx.x == 0) {
                    const unsigned int i = nbytes / 2 - 1;
                    const unsigned int v = rezero2(((const unsigned short*)x)[i], ((const unsigned short*)fx)[i], load_alpha(alpha));
                    ((unsigned short*)out)[i] = (unsigned short)v;
                }
            }
        } else {
            const unsigned int q = j - nfixed / 2;
            const char* wl = (const char*)rz_words + (((b >> 3) + (b >> 8)) & (kRep - 1)) * 64;
            const u64 v0 = *(cu64p)(uintptr_t)wl, v1 = *(cu64p)(uintptr_t)(wl + kRep * 64), v2 = *(cu64p)(uintptr_t)(wl + 2 * kRep * 64);
            unsigned int d0 = (unsigned int)v0, d1 = (unsigned int)v1, d2 = (unsigned int)v2;
            if (__builtin_expect(!((unsigned int)(v0 >> 32) == tagH && (unsigned int)(v1 >> 32) == tagH && (unsigned int)(v2 >> 32) == tagH &&
                                   ((d0 ^ tagL) | (d1 ^ tagL) | (d2 ^ tagL)) < 256), 0)) {
                gu64* my = (gu64*)(uintptr_t)(wl + (lane < 3 ? lane : 0) * kRep * 64);
                for (unsigned int spins = 0;; ++spins) {
                    u64 v = __hip_atomic_load(my, __ATOMIC_RELAXED, __HIP_MEMORY_SCOPE_AGENT);
                    const bool set = ((unsigned int)(v >> 32) == tagH) && (((unsigned int)v ^ tagL) < 256);
                    if (__all(set)) {
                        d0 = __builtin_amdgcn_readlane((unsigned int)v, 0);
                        d1 = __builtin_amdgcn_readlane((unsigned int)v, 1);
                        d2 = __builtin_amdgcn_readlane((unsigned int)v, 2);
                        break;
                    }
                    if (spins >= 32 && lane < 3 && !set)
                        __hip_atomic_compare_exchange_strong(my, &v, ((u64)tagH << 32) | tagL, __ATOMIC_RELAXED, __ATOMIC_RELAXED,
                                                             __HIP_MEMORY_SCOPE_AGENT);
                    __builtin_amdgcn_s_sleep(4);
                }
            }
            const unsigned int m = (d0 & d1) | (d0 & d2) | (d1 & d2);
            t = 2 * q + ((((b + (d0 >> 1)) >> 2) ^ m ^ (q >> 7)) & 1);
        }
    }

    const __amdgpu_buffer_rsrc_t rx = RZ_RSRC(x, nbytes), rf = RZ_RSRC(fx, nbytes), ro = RZ_RSRC(out, nbytes);
    const unsigned int off = t * 8192 + threadIdx.x * 16;
    const u32x4 x0 = __builtin_amdgcn_raw_buffer_load_b128(rx, off, 0, kLoadAux);
    const u32x4 f0 = __builtin_amdgcn_raw_buffer_load_b128(rf, off, 0, kLoadAux);
    const u32x4 x1 = __builtin_amdgcn_raw_buffer_load_b128(rx, off + 4096, 0, kLoadAux);
    const u32x4 f1 = __builtin_amdgcn_raw_buffer_load_b128(rf, off + 4096, 0, kLoadAux);
    __builtin_amdgcn_sched_barrier(0);
    const float a = load_alpha(alpha);
    __builtin_amdgcn_raw_buffer_store_b128(rezero8(x0, f0, a), ro, off, 0, kStoreAux);
    __builtin_amdgcn_raw_buffer_store_b128(rezero8(x1, f1, a), ro, off + 4096, 0, kStoreAux);
}

extern "C" void kernel_launch(void* const* d_in, const int* in_sizes, int n_in,
                              void* d_out, int out_size, void* d_ws, size_t ws_size,
                              hipStream_t stream) {
    const unsigned int nbytes = (unsigned int)out_size * 2u;
    const unsigned int npairs = nbytes >> 14;
    const unsigned int npad = (npairs + 3) / 4 * 8;
    const unsigned int nrem = ((nbytes & 16383u) + 8191u) / 8192u;
    const bool big = npairs >= kFixed / 2 + 256;
    const unsigned int nfixed = big ? kFixed : 0u, qfixed = big ? npairs - kFixed / 2 : 0u;
    rezero_kernel<<<kScouts + npad + nrem, kBlock, 0, stream>>>(d_in[0], d_in[1], d_out, (const unsigned short*)d_in[2], nbytes,
                                                                nfixed, qfixed, npairs);
}
```

```cpp
#include <hip/hip_runtime.h>
#include <hip/hip_bf16.h>
#include <stdint.h>

typedef unsigned long long u64;
typedef unsigned int u32x4 __attribute__((ext_vector_type(4)));
typedef unsigned int u32x16 __attribute__((ext_vector_type(16)));
typedef float f32x2 __attribute__((ext_vector_type(2)));
typedef __bf16 bf16x2 __attribute__((ext_vector_type(2)));
typedef __attribute__((address_space(1))) u64 gu64;
typedef const __attribute__((address_space(4))) unsigned int* cu32p;
typedef const __attribute__((address_space(4))) u64* cu64p;
typedef const __attribute__((address_space(4))) u32x16* cu32x16p;

constexpr unsigned int kBlock = 256;
#define P_LDS 24576
#define P_FIXED 1536
constexpr unsigned int kLdsBytes = P_LDS;
constexpr unsigned int kFixed = P_FIXED;
constexpr unsigned int kScouts = 32;
constexpr unsigned int kRep = 32;
constexpr int kRounds = 4;
constexpr int kLoadAux = 2;
#define P_HOLDALL 3
#define P_HOLDEARLY 30
#define P_HOLDCU 80
#define P_QMG 80
constexpr int kHoldAll = P_HOLDALL, kHoldEarly = P_HOLDEARLY, kHoldCu = P_HOLDCU;
constexpr int kStoreAux = 17;

__device__ u64 rz_words[(3 * kRep + 3 * kRounds) * 8];

extern "C" __device__ u64 rz_dispatch_id() __asm__("llvm.amdgcn.dispatch.id");

__device__ __forceinline__ unsigned int pack_bf16x2(float lo, float hi) {
    const f32x2 v = {lo, hi};
    return __builtin_bit_cast(unsigned int, __builtin_convertvector(v, bf16x2));
}
__device__ __forceinline__ unsigned int rezero2(unsigned int x, unsigned int f, float a) {
    const unsigned int t = pack_bf16x2(a * __builtin_bit_cast(float, f << 16), a * __builtin_bit_cast(float, f & 0xffff0000u));
    return pack_bf16x2(__builtin_bit_cast(float, x << 16) + __builtin_bit_cast(float, t << 16),
                       __builtin_bit_cast(float, x & 0xffff0000u) + __builtin_bit_cast(float, t & 0xffff0000u));
}
__device__ __forceinline__ u32x4 rezero8(u32x4 x, u32x4 f, float a) {
    u32x4 o;
    o.x = rezero2(x.x, f.x, a); o.y = rezero2(x.y, f.y, a); o.z = rezero2(x.z, f.z, a); o.w = rezero2(x.w, f.w, a);
    return o;
}
__device__ __forceinline__ float load_alpha(const unsigned short* alpha) {
    const uintptr_t p = (uintptr_t)alpha;
    const unsigned int w = *(cu32p)(p & ~(uintptr_t)3);
    return __builtin_bit_cast(float, (p & 2) ? (w & 0xffff0000u) : (w << 16));
}
__device__ __forceinline__ unsigned int xcc_id() {
    unsigned int v;
    asm volatile("s_getreg_b32 %0, hwreg(HW_REG_XCC_ID, 0, 3)" : "=s"(v));
    return v;
}
__device__ __forceinline__ void lds_barrier() {
    asm volatile("s_waitcnt lgkmcnt(0)\n\ts_barrier" ::: "memory");
}
__device__ __forceinline__ unsigned int timed_load(const char* a) {
    u64 t0, t1;
    unsigned int w;
    asm volatile("s_memtime %0\n\ts_waitcnt lgkmcnt(0)\n\ts_load_dword %1, %3, 0x0\n\ts_waitcnt lgkmcnt(0)\n\ts_memtime %2\n\ts_waitcnt lgkmcnt(0)"
                 : "=&s"(t0), "=&s"(w), "=&s"(t1) : "s"(a) : "memory");
    return (unsigned int)(t1 - t0);
}
#define RZ_RSRC(p, n) __builtin_amdgcn_make_buffer_rsrc((void*)(p), 0, (n), 0x00020000)

__global__ __launch_bounds__(kBlock) void rezero_kernel(const void* __restrict__ x, const void* __restrict__ fx,
                                                        void* __restrict__ out, const unsigned short* __restrict__ alpha,
                                                        unsigned int nbytes, unsigned int nfixed, unsigned int qfixed,
                                                        unsigned int npairs) {
    const unsigned int b0 = blockIdx.x;
    const unsigned int b = b0 - kScouts;
    const unsigned int j = (b >> 3) * 4 + (b & 3);
    unsigned int t;
    if (__builtin_expect(b < nfixed, 1)) {
        if (((b0 >> 3) & 31) < 3) __builtin_amdgcn_s_sleep(kHoldCu);
        else if (xcc_id() < 2) __builtin_amdgcn_s_sleep(kHoldEarly);
        else __builtin_amdgcn_s_sleep(kHoldAll);
        t = 2 * (j + qfixed) + ((b >> 2) & 1);
    } else {
        const unsigned int lane = threadIdx.x & 63;
        const u64 did = rz_dispatch_id();
        const unsigned int tagH = (unsigned int)(did >> 8) ^ 0x5AC3E1F7u, tagL = ((unsigned int)did << 24) | 0x00A5D200u;
        if (__builtin_expect(b0 < kScouts, 0)) {
            __shared__ unsigned int s_lat[kLdsBytes / 4];
            const unsigned int w = b0 >> 3;
            if (w == 3 || nfixed == 0) return;
            const unsigned int wave = __builtin_amdgcn_readfirstlane(threadIdx.x >> 6);
            gu64* my = (gu64*)rz_words + (w * kRep + lane) * 8;
            const unsigned int pg = b0 & 1;
            const char* p = (const char*)(w == 0 ? x : (w == 1 ? fx : (const void*)out)) + (pg << 21) + ((wave ^ (wave >> 1)) & 1) * 8192;
            unsigned int h = (unsigned int)did * 0x9E3779B1u + (b0 * 4 + wave) * 0x85EBCA6Bu;
            u64 old = 0;
            if (wave == 0 && lane < kRep) old = __hip_atomic_load(my, __ATOMIC_RELAXED, __HIP_MEMORY_SCOPE_AGENT);
            char* pool = (char*)rz_words + (3 * kRep + w * kRounds) * 64;
            const unsigned int xcc = xcc_id(), tagS = tagH ^ 0x3C5A96E7u;
            const bool evenNearIf0 = ((xcc >> 2) ^ pg) == 0;
            unsigned int lat = ~0u;
#pragma unroll
            for (int k = 0; k < kRounds; ++k) {
                const unsigned int l = timed_load(p + ((h >> 20) & 63) * 16384 + ((h >> 26) & 31) * 256);
                h = h * 0x2C1B3C6Du + 0x297A2D39u;
                lat = (l >= 380) ? min(lat, l) : lat;
                if (lane == 0) s_lat[wave] = lat;
                lds_barrier();
                const unsigned int l0 = __builtin_amdgcn_readfirstlane(s_lat[0]), l1 = __builtin_amdgcn_readfirstlane(s_lat[1]);
                const unsigned int l2 = __builtin_amdgcn_readfirstlane(s_lat[2]), l3 = __builtin_amdgcn_readfirstlane(s_lat[3]);
                const unsigned int la = min(min(l0, l3), 0xFFFFu), lb = min(min(l1, l2), 0xFFFFu);
                unsigned int u = evenNearIf0 ? la : lb, v = evenNearIf0 ? lb : la;
                if (threadIdx.x == 64)
                    __hip_atomic_store((gu64*)(uintptr_t)(pool + k * 64) + (b0 & 7), ((u64)tagS << 32) | (u << 16) | v, __ATOMIC_RELAXED,
                                       __HIP_MEMORY_SCOPE_AGENT);
                bool sure = min(u, v) < 800 && max(u, v) - min(u, v) >= 80 && max(u, v) != 0xFFFFu;
                if (k == 0) {
                    sure = sure || (max(u, v) - min(u, v) >= P_QMG && min(u, v) < 1500 && max(max(l0, l1), max(l2, l3)) != ~0u &&
                                    (l0 < l1) == (l3 < l2) && (l0 < l1) == (la < lb));
                } else if (!sure) {
                    const u32x16 o = *(cu32x16p)(uintptr_t)(pool + (k - 1) * 64);
                    unsigned int samples = 2 * k;
#pragma unroll
                    for (int i = 0; i < 8; ++i) {
                        const bool fresh = o[2 * i + 1] == tagS && i != (int)(b0 & 7);
                        u = fresh ? min(u, o[2 * i] >> 16) : u;
                        v = fresh ? min(v, o[2 * i] & 0xFFFFu) : v;
                        samples += (fresh && k > 1) ? 2 : 0;
                    }
                    sure = max(u, v) - min(u, v) >= (samples >= 6 ? 60u : 120u) && min(u, v) < 1100 && max(u, v) != 0xFFFFu;
                }
                if (sure || k == kRounds - 1) {
                    bool answered = false;
                    if (xcc >= 2 || k > 0) {
                        const u64 z = *(cu64p)(uintptr_t)((const char*)rz_words + (w * kRep + (b0 & 7) * 4 + k) * 64);
                        answered = (unsigned int)(z >> 32) == tagH && ((unsigned int)z ^ tagL) < 256;
                    }
                    u64 expected = old;
                    asm volatile("" : "+v"(expected));
                    const bool set = ((unsigned int)(expected >> 32) == tagH) && (((unsigned int)expected ^ tagL) < 256);
                    if (wave == 0 && lane < kRep && !set && !answered) {
                        const u64 want = ((u64)tagH << 32) | tagL | (((xcc - b0) & 7) << 1) | (u <= v ? 0u : 1u);
                        __hip_atomic_compare_exchange_strong(my, &expected, want, __ATOMIC_RELAXED, __ATOMIC_RELAXED, __HIP_MEMORY_SCOPE_AGENT);
                    }
                    break;
                }
                lds_barrier();
            }
            return;
        }
        if (__builtin_expect(j >= npairs || nfixed == 0, 0)) {
            const unsigned int npad = (npairs + 3) / 4 * 8;
            if (j < npairs && b < npad) {
                t = 2 * j + ((b >> 2) & 1);
            } else {
                if (b < npad) return;
                t = 2 * npairs + (b - npad);
                if ((nbytes & 2) && b == npad && threadIdx.x == 0) {
                    const unsigned int i = nbytes / 2 - 1;
                    const unsigned int v = rezero2(((const unsigned short*)x)[i], ((const unsigned short*)fx)[i], load_alpha(alpha));
                    ((unsigned short*)out)[i] = (unsigned short)v;
                }
            }
        } else {
            const unsigned int q = j - nfixed / 2;
            const char* wl = (const char*)rz_words + (((b >> 3) + (b >> 8)) & (kRep - 1)) * 64;
            const u64 v0 = *(cu64p)(uintptr_t)wl, v1 = *(cu64p)(uintptr_t)(wl + kRep * 64), v2 = *(cu64p)(uintptr_t)(wl + 2 * kRep * 64);
            unsigned int d0 = (unsigned int)v0, d1 = (unsigned int)v1, d2 = (unsigned int)v2;
            if (__builtin_expect(!((unsigned int)(v0 >> 32) == tagH && (unsigned int)(v1 >> 32) == tagH && (unsigned int)(v2 >> 32) == tagH &&
                                   ((d0 ^ tagL) | (d1 ^ tagL) | (d2 ^ tagL)) < 256), 0)) {
                gu64* my = (gu64*)(uintptr_t)(wl + (lane < 3 ? lane : 0) * kRep * 64);
                for (unsigned int spins = 0;; ++spins) {
                    u64 v = __hip_atomic_load(my, __ATOMIC_RELAXED, __HIP_MEMORY_SCOPE_AGENT);
                    const bool set = ((unsigned int)(v >> 32) == tagH) && (((unsigned int)v ^ tagL) < 256);
                    if (__all(set)) {
                        d0 = __builtin_amdgcn_readlane((unsigned int)v, 0);
                        d1 = __builtin_amdgcn_readlane((unsigned int)v, 1);
                        d2 = __builtin_amdgcn_readlane((unsigned int)v, 2);
                        break;
                    }
                    if (spins >= 32 && lane < 3 && !set)
                        __hip_atomic_compare_exchange_strong(my, &v, ((u64)tagH << 32) | tagL, __ATOMIC_RELAXED, __ATOMIC_RELAXED,
                                                             __HIP_MEMORY_SCOPE_AGENT);
                    __builtin_amdgcn_s_sleep(4);
                }
            }
            const unsigned int m = (d0 & d1) | (d0 & d2) | (d1 & d2);
            t = 2 * q + ((((b + (d0 >> 1)) >> 2) ^ m ^ (q >> 7)) & 1);
        }
    }

    const __amdgpu_buffer_rsrc_t rx = RZ_RSRC(x, nbytes), rf = RZ_RSRC(fx, nbytes), ro = RZ_RSRC(out, nbytes);
    const unsigned int off = t * 8192 + threadIdx.x * 16;
    const u32x4 x0 = __builtin_amdgcn_raw_buffer_load_b128(rx, off, 0, kLoadAux);
    const u32x4 f0 = __builtin_amdgcn_raw_buffer_load_b128(rf, off, 0, kLoadAux);
    const u32x4 x1 = __builtin_amdgcn_raw_buffer_load_b128(rx, off + 4096, 0, kLoadAux);
    const u32x4 f1 = __builtin_amdgcn_raw_buffer_load_b128(rf, off + 4096, 0, kLoadAux);
    __builtin_amdgcn_sched_barrier(0);
    const float a = load_alpha(alpha);
    __builtin_amdgcn_raw_buffer_store_b128(rezero8(x0, f0, a), ro, off, 0, kStoreAux);
    __builtin_amdgcn_raw_buffer_store_b128(rezero8(x1, f1, a), ro, off + 4096, 0, kStoreAux);
}

extern "C" void kernel_launch(void* const* d_in, const int* in_sizes, int n_in,
                              void* d_out, int out_size, void* d_ws, size_t ws_size,
                              hipStream_t stream) {
    const unsigned int nbytes = (unsigned int)out_size * 2u;
    const unsigned int npairs = nbytes >> 14;
    const unsigned int npad = (npairs + 3) / 4 * 8;
    const unsigned int nrem = ((nbytes & 16383u) + 8191u) / 8192u;
    const bool big = npairs >= kFixed / 2 + 256;
    const unsigned int nfixed = big ? kFixed : 0u, qfixed = big ? npairs - kFixed / 2 : 0u;
    rezero_kernel<<<kScouts + npad + nrem, kBlock, 0, stream>>>(d_in[0], d_in[1], d_out, (const unsigned short*)d_in[2], nbytes,
                                                                nfixed, qfixed, npairs);
}
```

```cpp
#define FLIP 0
#include <hip/hip_runtime.h>
#include <hip/hip_bf16.h>
#include <stdint.h>
#define BLOCK 256
#define UNROLL 2
#define LD_AUX 2
#define ST_AUX 17
#define PM 0x15555
typedef unsigned int u32x4 __attribute__((ext_vector_type(4)));
typedef float f32x2 __attribute__((ext_vector_type(2)));
typedef __bf16 bf16x2 __attribute__((ext_vector_type(2)));
__device__ __forceinline__ unsigned int pack_bf16x2(float lo, float hi) {
    f32x2 v = {lo, hi}; bf16x2 b = __builtin_convertvector(v, bf16x2); return __builtin_bit_cast(unsigned int, b);
}
__device__ __forceinline__ unsigned int rezero2(unsigned int x, unsigned int f, float a) {
    float flo = __builtin_bit_cast(float, f << 16), fhi = __builtin_bit_cast(float, f & 0xffff0000u);
    unsigned int t = pack_bf16x2(a * flo, a * fhi);
    float tlo = __builtin_bit_cast(float, t << 16), thi = __builtin_bit_cast(float, t & 0xffff0000u);
    float xlo = __builtin_bit_cast(float, x << 16), xhi = __builtin_bit_cast(float, x & 0xffff0000u);
    return pack_bf16x2(xlo + tlo, xhi + thi);
}
__device__ __forceinline__ u32x4 rezero8(u32x4 x, u32x4 f, float a) {
    u32x4 o; o.x = rezero2(x.x, f.x, a); o.y = rezero2(x.y, f.y, a); o.z = rezero2(x.z, f.z, a); o.w = rezero2(x.w, f.w, a); return o;
}
__global__ __launch_bounds__(BLOCK) void rezero_kernel(const void* __restrict__ x, const void* __restrict__ fx,
        const unsigned short* __restrict__ alpha, void* __restrict__ out, unsigned int nbytes, void* __restrict__ unused_ws) {
    uintptr_t p = (uintptr_t)alpha;
    typedef const __attribute__((address_space(4))) unsigned int* cptr;
    unsigned int w = *(cptr)(p & ~(uintptr_t)3);
    unsigned int h = (p & 2) ? (w & 0xffff0000u) : (w << 16);
    const float a = __builtin_bit_cast(float, h);
    __amdgpu_buffer_rsrc_t rx = __builtin_amdgcn_make_buffer_rsrc((void*)x, 0, nbytes, 0x00020000);
    __amdgpu_buffer_rsrc_t rf = __builtin_amdgcn_make_buffer_rsrc((void*)fx, 0, nbytes, 0x00020000);
    __amdgpu_buffer_rsrc_t ro = __builtin_amdgcn_make_buffer_rsrc((void*)out, 0, nbytes, 0x00020000);
    unsigned int b = blockIdx.x, t;
    if (FLIP == 2) t = b;
    else {
        unsigned int xcc; asm volatile("s_getreg_b32 %0, hwreg(HW_REG_XCC_ID)" : "=s"(xcc)); xcc &= 7;
        unsigned int iod = xcc >> 2;
        unsigned int q = (b >> 3) * 4 + (xcc & 3);
        unsigned int par = (PM >> (q >> 7)) & 1;
        t = 2 * q + (iod ^ par ^ FLIP);
    }
    unsigned int off = t * 8192 + threadIdx.x * 16;
    u32x4 xv[UNROLL], fv[UNROLL];
#pragma unroll
    for (int u = 0; u < UNROLL; ++u) {
        xv[u] = __builtin_amdgcn_raw_buffer_load_b128(rx, off + u * BLOCK * 16, 0, LD_AUX);
        fv[u] = __builtin_amdgcn_raw_buffer_load_b128(rf, off + u * BLOCK * 16, 0, LD_AUX);
    }
#pragma unroll
    for (int u = 0; u < UNROLL; ++u)
        __builtin_amdgcn_raw_buffer_store_b128(rezero8(xv[u], fv[u], a), ro, off + u * BLOCK * 16, 0, ST_AUX);
}
extern "C" void kernel_launch(void* const* d_in, const int* in_sizes, int n_in,
                              void* d_out, int out_size, void* d_ws, size_t ws_size,
                              hipStream_t stream) {
    const unsigned int nbytes = (unsigned int)out_size * 2u;
    rezero_kernel<<<nbytes / 8192, BLOCK, 0, stream>>>(d_in[0], d_in[1], (const unsigned short*)d_in[2], d_out, nbytes, d_ws);
}
```

```cpp
#include <hip/hip_runtime.h>
#include <hip/hip_bf16.h>
#include <stdint.h>

typedef unsigned long long u64;
typedef unsigned int u32x4 __attribute__((ext_vector_type(4)));
typedef unsigned int u32x16 __attribute__((ext_vector_type(16)));
typedef float f32x2 __attribute__((ext_vector_type(2)));
typedef __bf16 bf16x2 __attribute__((ext_vector_type(2)));
typedef __attribute__((address_space(1))) u64 gu64;
typedef const __attribute__((address_space(4))) unsigned int* cu32p;
typedef const __attribute__((address_space(4))) u64* cu64p;
typedef const __attribute__((address_space(4))) u32x16* cu32x16p;

constexpr unsigned int kBlock = 256;
#define P_LDS 24576
#define P_FIXED 1536
constexpr unsigned int kLdsBytes = P_LDS;
constexpr unsigned int kFixed = P_FIXED;
constexpr unsigned int kScouts = 32;
constexpr unsigned int kRep = 32;
constexpr int kRounds = 4;
constexpr int kLoadAux = 2;
#define P_HOLDALL 8
#define P_HOLDEARLY 30
#define P_HOLDCU 80
#define P_QMG 80
constexpr int kHoldAll = P_HOLDALL, kHoldEarly = P_HOLDEARLY, kHoldCu = P_HOLDCU;
constexpr int kStoreAux = 17;

__device__ u64 rz_words[(3 * kRep + 3 * kRounds) * 8];

extern "C" __device__ u64 rz_dispatch_id() __asm__("llvm.amdgcn.dispatch.id");

__device__ __forceinline__ unsigned int pack_bf16x2(float lo, float hi) {
    const f32x2 v = {lo, hi};
    return __builtin_bit_cast(unsigned int, __builtin_convertvector(v, bf16x2));
}
__device__ __forceinline__ unsigned int rezero2(unsigned int x, unsigned int f, float a) {
    const unsigned int t = pack_bf16x2(a * __builtin_bit_cast(float, f << 16), a * __builtin_bit_cast(float, f & 0xffff0000u));
    return pack_bf16x2(__builtin_bit_cast(float, x << 16) + __builtin_bit_cast(float, t << 16),
                       __builtin_bit_cast(float, x & 0xffff0000u) + __builtin_bit_cast(float, t & 0xffff0000u));
}
__device__ __forceinline__ u32x4 rezero8(u32x4 x, u32x4 f, float a) {
    u32x4 o;
    o.x = rezero2(x.x, f.x, a); o.y = rezero2(x.y, f.y, a); o.z = rezero2(x.z, f.z, a); o.w = rezero2(x.w, f.w, a);
    return o;
}
__device__ __forceinline__ float load_alpha(const unsigned short* alpha) {
    const uintptr_t p = (uintptr_t)alpha;
    const unsigned int w = *(cu32p)(p & ~(uintptr_t)3);
    return __builtin_bit_cast(float, (p & 2) ? (w & 0xffff0000u) : (w << 16));
}
__device__ __forceinline__ unsigned int xcc_id() {
    unsigned int v;
    asm volatile("s_getreg_b32 %0, hwreg(HW_REG_XCC_ID, 0, 3)" : "=s"(v));
    return v;
}
__device__ __forceinline__ void lds_barrier() {
    asm volatile("s_waitcnt lgkmcnt(0)\n\ts_barrier" ::: "memory");
}
__device__ __forceinline__ unsigned int timed_load(const char* a) {
    u64 t0, t1;
    unsigned int w;
    asm volatile("s_memtime %0\n\ts_waitcnt lgkmcnt(0)\n\ts_load_dword %1, %3, 0x0\n\ts_waitcnt lgkmcnt(0)\n\ts_memtime %2\n\ts_waitcnt lgkmcnt(0)"
                 : "=&s"(t0), "=&s"(w), "=&s"(t1) : "s"(a) : "memory");
    return (unsigned int)(t1 - t0);
}
#define RZ_RSRC(p, n) __builtin_amdgcn_make_buffer_rsrc((void*)(p), 0, (n), 0x00020000)

__global__ __launch_bounds__(kBlock) void rezero_kernel(const void* __restrict__ x, const void* __restrict__ fx,
                                                        void* __restrict__ out, const unsigned short* __restrict__ alpha,
                                                        unsigned int nbytes, unsigned int nfixed, unsigned int qfixed,
                                                        unsigned int npairs, const void* ws) {
    const unsigned int b0 = blockIdx.x;
    const unsigned int b = b0 - kScouts;
    const unsigned int j = (b >> 3) * 4 + (b & 3);
    unsigned int t;
    if (__builtin_expect(b < nfixed, 1)) {
        if (((b0 >> 3) & 31) < 3) __builtin_amdgcn_s_sleep(kHoldCu);
        else if (xcc_id() < 2) __builtin_amdgcn_s_sleep(kHoldEarly);
        else __builtin_amdgcn_s_sleep(kHoldAll);
        t = 2 * (j + qfixed) + ((b >> 2) & 1);
    } else {
        const unsigned int lane = threadIdx.x & 63;
        const u64 did = rz_dispatch_id();
        const unsigned int tagH = (unsigned int)(did >> 8) ^ 0x5AC3E1F7u, tagL = ((unsigned int)did << 24) | 0x00A5D200u;
        if (__builtin_expect(b0 < kScouts, 0)) {
            __shared__ unsigned int s_lat[kLdsBytes / 4];
            const unsigned int w = b0 >> 3;
            if (w == 3 || nfixed == 0) return;
            const unsigned int wave = __builtin_amdgcn_readfirstlane(threadIdx.x >> 6);
            gu64* my = (gu64*)rz_words + (w * kRep + lane) * 8;
            const unsigned int pg = b0 & 1;
            const char* p = (const char*)(w == 0 ? x : (w == 1 ? fx : (const void*)out)) + (pg << 21) + ((wave ^ (wave >> 1)) & 1) * 8192;
            unsigned int h = (unsigned int)did * 0x9E3779B1u + (b0 * 4 + wave) * 0x85EBCA6Bu;
            u64 old = 0;
            if (wave == 0 && lane < kRep) old = __hip_atomic_load(my, __ATOMIC_RELAXED, __HIP_MEMORY_SCOPE_AGENT);
            char* pool = (char*)rz_words + (3 * kRep + w * kRounds) * 64;
            const unsigned int xcc = xcc_id(), tagS = tagH ^ 0x3C5A96E7u;
            const bool evenNearIf0 = ((xcc >> 2) ^ pg) == 0;
            unsigned int lat = ~0u, u0 = 0xFFFFu, v0 = 0xFFFFu;
#pragma unroll
            for (int k = 0; k < kRounds; ++k) {
                const unsigned int l = timed_load(p + ((h >> 20) & 63) * 16384 + ((h >> 26) & 31) * 256);
                h = h * 0x2C1B3C6Du + 0x297A2D39u;
                lat = (l >= 380) ? min(lat, l) : lat;
                if (lane == 0) s_lat[wave] = lat;
                lds_barrier();
                const unsigned int l0 = __builtin_amdgcn_readfirstlane(s_lat[0]), l1 = __builtin_amdgcn_readfirstlane(s_lat[1]);
                const unsigned int l2 = __builtin_amdgcn_readfirstlane(s_lat[2]), l3 = __builtin_amdgcn_readfirstlane(s_lat[3]);
                const unsigned int la = min(min(l0, l3), 0xFFFFu), lb = min(min(l1, l2), 0xFFFFu);
                unsigned int u = evenNearIf0 ? la : lb, v = evenNearIf0 ? lb : la;
                if (threadIdx.x == 64)
                    __hip_atomic_store((gu64*)(uintptr_t)(pool + k * 64) + (b0 & 7), ((u64)tagS << 32) | (u << 16) | v, __ATOMIC_RELAXED,
                                       __HIP_MEMORY_SCOPE_AGENT);
                bool sure = min(u, v) < 800 && max(u, v) - min(u, v) >= 80 && max(u, v) != 0xFFFFu;
                if (k == 0) {
                    u0 = u; v0 = v;
                    sure = sure || (w != 2 && max(u, v) - min(u, v) >= P_QMG && min(u, v) < 1500 && max(max(l0, l1), max(l2, l3)) != ~0u &&
                                    (l0 < l1) == (l3 < l2) && (l0 < l1) == (la < lb));
                } else if (!sure) {
                    const u32x16 o = *(cu32x16p)(uintptr_t)(pool + (k - 1) * 64);
                    unsigned int samples = 2 * k, others = 0;
                    unsigned int pu = 0xFFFFu, pv = 0xFFFFu;
#pragma unroll
                    for (int i = 0; i < 8; ++i) {
                        const bool fresh = o[2 * i + 1] == tagS && i != (int)(b0 & 7);
                        pu = fresh ? min(pu, o[2 * i] >> 16) : pu;
                        pv = fresh ? min(pv, o[2 * i] & 0xFFFFu) : pv;
                        others += fresh ? 1 : 0;
                    }
                    if (k == 1) {
                        const unsigned int fu = min(pu, u0), fv = min(pv, v0), fd = max(fu, fv) - min(fu, fv);
                        const bool valid = max(fu, fv) != 0xFFFFu && max(u, v) != 0xFFFFu;
                        if (valid && others >= 3 && fd >= 80) { sure = true; u = fu; v = fv; }
                        else sure = valid && max(u, v) - min(u, v) >= 120 && min(u, v) < 1100 && (w != 2 || (others >= 1 && fd >= 40 && (fu < fv) == (u < v)));
                    } else {
                        u = min(u, pu); v = min(v, pv);
                        samples += 2 * others;
                        sure = max(u, v) - min(u, v) >= (samples >= 6 ? 60u : 120u) && min(u, v) < 1100 && max(u, v) != 0xFFFFu;
                    }
                }
                if (sure || k == kRounds - 1) {
                    bool answered = false;
                    if (xcc >= 2 || k > 0) {
                        const u64 z = *(cu64p)(uintptr_t)((const char*)rz_words + (w * kRep + (b0 & 7) * 4 + k) * 64);
                        answered = (unsigned int)(z >> 32) == tagH && ((unsigned int)z ^ tagL) < 256;
                    }
                    u64 expected = old;
                    asm volatile("" : "+v"(expected));
                    const bool set = ((unsigned int)(expected >> 32) == tagH) && (((unsigned int)expected ^ tagL) < 256);
                    if (wave == 0 && lane < kRep && !set && !answered) {
                        const u64 want = ((u64)tagH << 32) | tagL | (((xcc - b0) & 7) << 1) | (u <= v ? 0u : 1u);
                        __hip_atomic_compare_exchange_strong(my, &expected, want, __ATOMIC_RELAXED, __ATOMIC_RELAXED, __HIP_MEMORY_SCOPE_AGENT);
                    }
                    break;
                }
                lds_barrier();
            }
            return;
        }
        if (__builtin_expect(j >= npairs || nfixed == 0, 0)) {
            const unsigned int npad = (npairs + 3) / 4 * 8;
            if (j < npairs && b < npad) {
                t = 2 * j + ((b >> 2) & 1);
            } else {
                if (b < npad) return;
                t = 2 * npairs + (b - npad);
                if ((nbytes & 2) && b == npad && threadIdx.x == 0) {
                    const unsigned int i = nbytes / 2 - 1;
                    const unsigned int v = rezero2(((const unsigned short*)x)[i], ((const unsigned short*)fx)[i], load_alpha(alpha));
                    ((unsigned short*)out)[i] = (unsigned short)v;
                }
            }
        } else {
            const unsigned int q = j - nfixed / 2;
            const char* wl = (const char*)rz_words + (((b >> 3) + (b >> 8)) & (kRep - 1)) * 64;
            const u64 v0 = *(cu64p)(uintptr_t)wl, v1 = *(cu64p)(uintptr_t)(wl + kRep * 64), v2 = *(cu64p)(uintptr_t)(wl + 2 * kRep * 64);
            unsigned int d0 = (unsigned int)v0, d1 = (unsigned int)v1, d2 = (unsigned int)v2;
            if (__builtin_expect(!((unsigned int)(v0 >> 32) == tagH && (unsigned int)(v1 >> 32) == tagH && ((d0 ^ tagL) | (d1 ^ tagL)) < 256 &&
                                   (((d0 ^ d1) & 1) == 0 || ((unsigned int)(v2 >> 32) == tagH && (d2 ^ tagL) < 256))), 0)) {
                gu64* my = (gu64*)(uintptr_t)(wl + (lane < 3 ? lane : 0) * kRep * 64);
                for (unsigned int spins = 0;; ++spins) {
                    u64 v = __hip_atomic_load(my, __ATOMIC_RELAXED, __HIP_MEMORY_SCOPE_AGENT);
                    const bool set = ((unsigned int)(v >> 32) == tagH) && (((unsigned int)v ^ tagL) < 256);
                    const u64 mask = __ballot(set);
                    d0 = __builtin_amdgcn_readlane((unsigned int)v, 0);
                    d1 = __builtin_amdgcn_readlane((unsigned int)v, 1);
                    d2 = __builtin_amdgcn_readlane((unsigned int)v, 2);
                    if ((mask & 3) == 3 && (((d0 ^ d1) & 1) == 0 || (mask & 4))) break;
                    if (spins >= 32 && lane < 3 && !set)
                        __hip_atomic_compare_exchange_strong(my, &v, ((u64)tagH << 32) | tagL, __ATOMIC_RELAXED, __ATOMIC_RELAXED,
                                                             __HIP_MEMORY_SCOPE_AGENT);
                    __builtin_amdgcn_s_sleep(4);
                }
            }
            const unsigned int m = ((d0 ^ d1) & 1) ? d2 : d0;
            t = 2 * q + ((((b + (d0 >> 1)) >> 2) ^ m ^ (q >> 7)) & 1);
        }
    }

    const __amdgpu_buffer_rsrc_t rx = RZ_RSRC(x, nbytes), rf = RZ_RSRC(fx, nbytes), ro = RZ_RSRC(out, nbytes);
    const unsigned int off = t * 8192 + threadIdx.x * 16;
    const u32x4 x0 = __builtin_amdgcn_raw_buffer_load_b128(rx, off, 0, kLoadAux);
    const u32x4 f0 = __builtin_amdgcn_raw_buffer_load_b128(rf, off, 0, kLoadAux);
    const u32x4 x1 = __builtin_amdgcn_raw_buffer_load_b128(rx, off + 4096, 0, kLoadAux);
    const u32x4 f1 = __builtin_amdgcn_raw_buffer_load_b128(rf, off + 4096, 0, kLoadAux);
    __builtin_amdgcn_sched_barrier(0);
    const float a = load_alpha(alpha);
    __builtin_amdgcn_raw_buffer_store_b128(rezero8(x0, f0, a), ro, off, 0, kStoreAux);
    __builtin_amdgcn_raw_buffer_store_b128(rezero8(x1, f1, a), ro, off + 4096, 0, kStoreAux);
}

extern "C" void kernel_launch(void* const* d_in, const int* in_sizes, int n_in,
                              void* d_out, int out_size, void* d_ws, size_t ws_size,
                              hipStream_t stream) {
    const unsigned int nbytes = (unsigned int)out_size * 2u;
    const unsigned int npairs = nbytes >> 14;
    const unsigned int npad = (npairs + 3) / 4 * 8;
    const unsigned int nrem = ((nbytes & 16383u) + 8191u) / 8192u;
    const bool big = npairs >= kFixed / 2 + 256;
    const unsigned int nfixed = big ? kFixed : 0u, qfixed = big ? npairs - kFixed / 2 : 0u;
    rezero_kernel<<<kScouts + npad + nrem, kBlock, 0, stream>>>(d_in[0], d_in[1], d_out, (const unsigned short*)d_in[2], nbytes,
                                                                nfixed, qfixed, npairs, d_ws);
}
```

```cpp
#include <hip/hip_runtime.h>
#include <hip/hip_bf16.h>
#include <stdint.h>

typedef unsigned long long u64;
typedef unsigned int u32x4 __attribute__((ext_vector_type(4)));
typedef unsigned int u32x16 __attribute__((ext_vector_type(16)));
typedef float f32x2 __attribute__((ext_vector_type(2)));
typedef __bf16 bf16x2 __attribute__((ext_vector_type(2)));
typedef __attribute__((address_space(1))) u64 gu64;
typedef const __attribute__((address_space(4))) unsigned int* cu32p;
typedef const __attribute__((address_space(4))) u64* cu64p;
typedef const __attribute__((address_space(4))) u32x16* cu32x16p;

constexpr unsigned int kBlock = 256;
#define P_LDS 24576
#define P_FIXED 1536
constexpr unsigned int kLdsBytes = P_LDS;
constexpr unsigned int kFixed = P_FIXED;
constexpr unsigned int kScouts = 32;
constexpr unsigned int kRep = 32;
constexpr int kRounds = 4;
constexpr int kLoadAux = 2;
#define P_HOLDALL 8
#define P_HOLDEARLY 30
#define P_HOLDCU 80
#define P_QMG 80
constexpr int kHoldAll = P_HOLDALL, kHoldEarly = P_HOLDEARLY, kHoldCu = P_HOLDCU;
constexpr int kStoreAux = 17;

__device__ u64 rz_words[(3 * kRep + 3 * kRounds) * 8];

extern "C" __device__ u64 rz_dispatch_id() __asm__("llvm.amdgcn.dispatch.id");

__device__ __forceinline__ unsigned int pack_bf16x2(float lo, float hi) {
    const f32x2 v = {lo, hi};
    return __builtin_bit_cast(unsigned int, __builtin_convertvector(v, bf16x2));
}
__device__ __forceinline__ unsigned int rezero2(unsigned int x, unsigned int f, float a) {
    const unsigned int t = pack_bf16x2(a * __builtin_bit_cast(float, f << 16), a * __builtin_bit_cast(float, f & 0xffff0000u));
    return pack_bf16x2(__builtin_bit_cast(float, x << 16) + __builtin_bit_cast(float, t << 16),
                       __builtin_bit_cast(float, x & 0xffff0000u) + __builtin_bit_cast(float, t & 0xffff0000u));
}
__device__ __forceinline__ u32x4 rezero8(u32x4 x, u32x4 f, float a) {
    u32x4 o;
    o.x = rezero2(x.x, f.x, a); o.y = rezero2(x.y, f.y, a); o.z = rezero2(x.z, f.z, a); o.w = rezero2(x.w, f.w, a);
    return o;
}
__device__ __forceinline__ float load_alpha(const unsigned short* alpha) {
    const uintptr_t p = (uintptr_t)alpha;
    const unsigned int w = *(cu32p)(p & ~(uintptr_t)3);
    return __builtin_bit_cast(float, (p & 2) ? (w & 0xffff0000u) : (w << 16));
}
__device__ __forceinline__ unsigned int xcc_id() {
    unsigned int v;
    asm volatile("s_getreg_b32 %0, hwreg(HW_REG_XCC_ID, 0, 3)" : "=s"(v));
    return v;
}
__device__ __forceinline__ void lds_barrier() {
    asm volatile("s_waitcnt lgkmcnt(0)\n\ts_barrier" ::: "memory");
}
__device__ __forceinline__ unsigned int timed_load(const char* a) {
    u64 t0, t1;
    unsigned int w;
    asm volatile("s_memtime %0\n\ts_waitcnt lgkmcnt(0)\n\ts_load_dword %1, %3, 0x0\n\ts_waitcnt lgkmcnt(0)\n\ts_memtime %2\n\ts_waitcnt lgkmcnt(0)"
                 : "=&s"(t0), "=&s"(w), "=&s"(t1) : "s"(a) : "memory");
    return (unsigned int)(t1 - t0);
}
#define RZ_RSRC(p, n) __builtin_amdgcn_make_buffer_rsrc((void*)(p), 0, (n), 0x00020000)

__global__ __launch_bounds__(kBlock) void rezero_kernel(const void* __restrict__ x, const void* __restrict__ fx,
                                                        void* __restrict__ out, const unsigned short* __restrict__ alpha,
                                                        unsigned int nbytes, unsigned int nfixed, unsigned int qfixed,
                                                        unsigned int npairs, const void* ws) {
    const unsigned int b0 = blockIdx.x;
    const unsigned int b = b0 - kScouts;
    const unsigned int j = (b >> 3) * 4 + (b & 3);
    unsigned int t;
    if (__builtin_expect(b < nfixed, 1)) {
        if (((b0 >> 3) & 31) < 3) __builtin_amdgcn_s_sleep(kHoldCu);
        else if (xcc_id() < 2) __builtin_amdgcn_s_sleep(kHoldEarly);
        else __builtin_amdgcn_s_sleep(kHoldAll);
        t = 2 * (j + qfixed) + ((b >> 2) & 1);
    } else {
        const unsigned int lane = threadIdx.x & 63;
        const u64 did = rz_dispatch_id();
        const unsigned int tagH = (unsigned int)(did >> 8) ^ 0x5AC3E1F7u, tagL = ((unsigned int)did << 24) | 0x00A5D200u;
        if (__builtin_expect(b0 < kScouts, 0)) {
            __shared__ unsigned int s_lat[kLdsBytes / 4];
            const unsigned int w = b0 >> 3;
            if (w == 3 || nfixed == 0) return;
            const unsigned int wave = __builtin_amdgcn_readfirstlane(threadIdx.x >> 6);
            gu64* my = (gu64*)rz_words + (w * kRep + lane) * 8;
            const unsigned int pg = b0 & 1;
            const char* p = (const char*)(w == 0 ? x : (w == 1 ? fx : (const void*)out)) + (pg << 21) + ((wave ^ (wave >> 1)) & 1) * 8192;
            unsigned int h = (unsigned int)did * 0x9E3779B1u + (b0 * 4 + wave) * 0x85EBCA6Bu;
            u64 old = 0;
            if (wave == 0 && lane < kRep) old = __hip_atomic_load(my, __ATOMIC_RELAXED, __HIP_MEMORY_SCOPE_AGENT);
            char* pool = (char*)rz_words + (3 * kRep + w * kRounds) * 64;
            const unsigned int xcc = xcc_id(), tagS = tagH ^ 0x3C5A96E7u;
            const bool evenNearIf0 = ((xcc >> 2) ^ pg) == 0;
            unsigned int lat = ~0u;
#pragma unroll
            for (int k = 0; k < kRounds; ++k) {
                const unsigned int l = timed_load(p + ((h >> 20) & 63) * 16384 + ((h >> 26) & 31) * 256);
                h = h * 0x2C1B3C6Du + 0x297A2D39u;
                lat = (l >= 380) ? min(lat, l) : lat;
                if (lane == 0) s_lat[wave] = lat;
                lds_barrier();
                const unsigned int l0 = __builtin_amdgcn_readfirstlane(s_lat[0]), l1 = __builtin_amdgcn_readfirstlane(s_lat[1]);
                const unsigned int l2 = __builtin_amdgcn_readfirstlane(s_lat[2]), l3 = __builtin_amdgcn_readfirstlane(s_lat[3]);
                const unsigned int la = min(min(l0, l3), 0xFFFFu), lb = min(min(l1, l2), 0xFFFFu);
                unsigned int u = evenNearIf0 ? la : lb, v = evenNearIf0 ? lb : la;
                if (threadIdx.x == 64)
                    __hip_atomic_store((gu64*)(uintptr_t)(pool + k * 64) + (b0 & 7), ((u64)tagS << 32) | (u << 16) | v, __ATOMIC_RELAXED,
                                       __HIP_MEMORY_SCOPE_AGENT);
                bool sure = min(u, v) < 800 && max(u, v) - min(u, v) >= 80 && max(u, v) != 0xFFFFu;
                if (k == 0) {
                    sure = sure || (max(u, v) - min(u, v) >= P_QMG && min(u, v) < 1500 && max(max(l0, l1), max(l2, l3)) != ~0u &&
                                    (l0 < l1) == (l3 < l2) && (l0 < l1) == (la < lb));
                } else if (!sure) {
                    const u32x16 o = *(cu32x16p)(uintptr_t)(pool + (k - 1) * 64);
                    unsigned int samples = 2 * k;
#pragma unroll
                    for (int i = 0; i < 8; ++i) {
                        const bool fresh = o[2 * i + 1] == tagS && i != (int)(b0 & 7);
                        u = fresh ? min(u, o[2 * i] >> 16) : u;
                        v = fresh ? min(v, o[2 * i] & 0xFFFFu) : v;
                        samples += (fresh && k > 1) ? 2 : 0;
                    }
                    sure = max(u, v) - min(u, v) >= (samples >= 6 ? 60u : 120u) && min(u, v) < 1100 && max(u, v) != 0xFFFFu;
                }
                if (sure || k == kRounds - 1) {
                    bool answered = false;
                    if (xcc >= 2 || k > 0) {
                        const u64 z = *(cu64p)(uintptr_t)((const char*)rz_words + (w * kRep + (b0 & 7) * 4 + k) * 64);
                        answered = (unsigned int)(z >> 32) == tagH && ((unsigned int)z ^ tagL) < 256;
                    }
                    u64 expected = old;
                    asm volatile("" : "+v"(expected));
                    const bool set = ((unsigned int)(expected >> 32) == tagH) && (((unsigned int)expected ^ tagL) < 256);
                    if (wave == 0 && lane < kRep && !set && !answered) {
                        const u64 want = ((u64)tagH << 32) | tagL | (((xcc - b0) & 7) << 1) | (u <= v ? 0u : 1u);
                        __hip_atomic_compare_exchange_strong(my, &expected, want, __ATOMIC_RELAXED, __ATOMIC_RELAXED, __HIP_MEMORY_SCOPE_AGENT);
                    }
                    break;
                }
                lds_barrier();
            }
            return;
        }
        if (__builtin_expect(j >= npairs || nfixed == 0, 0)) {
            const unsigned int npad = (npairs + 3) / 4 * 8;
            if (j < npairs && b < npad) {
                t = 2 * j + ((b >> 2) & 1);
            } else {
                if (b < npad) return;
                t = 2 * npairs + (b - npad);
                if ((nbytes & 2) && b == npad && threadIdx.x == 0) {
                    const unsigned int i = nbytes / 2 - 1;
                    const unsigned int v = rezero2(((const unsigned short*)x)[i], ((const unsigned short*)fx)[i], load_alpha(alpha));
                    ((unsigned short*)out)[i] = (unsigned short)v;
                }
            }
        } else {
            const unsigned int q = j - nfixed / 2;
            const char* wl = (const char*)rz_words + (((b >> 3) + (b >> 8)) & (kRep - 1)) * 64;
            const u64 v0 = *(cu64p)(uintptr_t)wl, v1 = *(cu64p)(uintptr_t)(wl + kRep * 64), v2 = *(cu64p)(uintptr_t)(wl + 2 * kRep * 64);
            unsigned int d0 = (unsigned int)v0, d1 = (unsigned int)v1, d2 = (unsigned int)v2;
            if (__builtin_expect(!((unsigned int)(v0 >> 32) == tagH && (unsigned int)(v1 >> 32) == tagH && ((d0 ^ tagL) | (d1 ^ tagL)) < 256 &&
                                   (((d0 ^ d1) & 1) == 0 || ((unsigned int)(v2 >> 32) == tagH && (d2 ^ tagL) < 256))), 0)) {
                gu64* my = (gu64*)(uintptr_t)(wl + (lane < 3 ? lane : 0) * kRep * 64);
                for (unsigned int spins = 0;; ++spins) {
                    u64 v = __hip_atomic_load(my, __ATOMIC_RELAXED, __HIP_MEMORY_SCOPE_AGENT);
                    const bool set = ((unsigned int)(v >> 32) == tagH) && (((unsigned int)v ^ tagL) < 256);
                    const u64 mask = __ballot(set);
                    d0 = __builtin_amdgcn_readlane((unsigned int)v, 0);
                    d1 = __builtin_amdgcn_readlane((unsigned int)v, 1);
                    d2 = __builtin_amdgcn_readlane((unsigned int)v, 2);
                    if ((mask & 3) == 3 && (((d0 ^ d1) & 1) == 0 || (mask & 4))) break;
                    if (spins >= 32 && lane < 3 && !set)
                        __hip_atomic_compare_exchange_strong(my, &v, ((u64)tagH << 32) | tagL, __ATOMIC_RELAXED, __ATOMIC_RELAXED,
                                                             __HIP_MEMORY_SCOPE_AGENT);
                    __builtin_amdgcn_s_sleep(4);
                }
            }
            const unsigned int m = ((d0 ^ d1) & 1) ? d2 : d0;
            t = 2 * q + ((((b + (d0 >> 1)) >> 2) ^ m ^ (q >> 7)) & 1);
        }
    }

    const __amdgpu_buffer_rsrc_t rx = RZ_RSRC(x, nbytes), rf = RZ_RSRC(fx, nbytes), ro = RZ_RSRC(out, nbytes);
    const unsigned int off = t * 8192 + threadIdx.x * 16;
    const u32x4 x0 = __builtin_amdgcn_raw_buffer_load_b128(rx, off, 0, kLoadAux);
    const u32x4 f0 = __builtin_amdgcn_raw_buffer_load_b128(rf, off, 0, kLoadAux);
    const u32x4 x1 = __builtin_amdgcn_raw_buffer_load_b128(rx, off + 4096, 0, kLoadAux);
    const u32x4 f1 = __builtin_amdgcn_raw_buffer_load_b128(rf, off + 4096, 0, kLoadAux);
    __builtin_amdgcn_sched_barrier(0);
    const float a = load_alpha(alpha);
    __builtin_amdgcn_raw_buffer_store_b128(rezero8(x0, f0, a), ro, off, 0, kStoreAux);
    __builtin_amdgcn_raw_buffer_store_b128(rezero8(x1, f1, a), ro, off + 4096, 0, kStoreAux);
}

extern "C" void kernel_launch(void* const* d_in, const int* in_sizes, int n_in,
                              void* d_out, int out_size, void* d_ws, size_t ws_size,
                              hipStream_t stream) {
    const unsigned int nbytes = (unsigned int)out_size * 2u;
    const unsigned int npairs = nbytes >> 14;
    const unsigned int npad = (npairs + 3) / 4 * 8;
    const unsigned int nrem = ((nbytes & 16383u) + 8191u) / 8192u;
    const bool big = npairs >= kFixed / 2 + 256;
    const unsigned int nfixed = big ? kFixed : 0u, qfixed = big ? npairs - kFixed / 2 : 0u;
    rezero_kernel<<<kScouts + npad + nrem, kBlock, 0, stream>>>(d_in[0], d_in[1], d_out, (const unsigned short*)d_in[2], nbytes,
                                                                nfixed, qfixed, npairs, d_ws);
}
```

```cpp
#include <hip/hip_runtime.h>
#include <hip/hip_bf16.h>
#include <stdint.h>

typedef unsigned long long u64;
typedef unsigned int u32x4 __attribute__((ext_vector_type(4)));
typedef unsigned int u32x16 __attribute__((ext_vector_type(16)));
typedef float f32x2 __attribute__((ext_vector_type(2)));
typedef __bf16 bf16x2 __attribute__((ext_vector_type(2)));
typedef __attribute__((address_space(1))) u64 gu64;
typedef const __attribute__((address_space(4))) unsigned int* cu32p;
typedef const __attribute__((address_space(4))) u64* cu64p;
typedef const __attribute__((address_space(4))) u32x16* cu32x16p;

constexpr unsigned int kBlock = 256;
constexpr unsigned int kLdsBytes = 24576;
constexpr unsigned int kFixed = 1536;
constexpr unsigned int kScouts = 32;
constexpr unsigned int kRep = 32;
constexpr int kRounds = 4;
constexpr int kLoadAux = 2;
constexpr int kHoldAll = 8, kHoldEarly = 30, kHoldCu = 80;
constexpr int kStoreAux = 17;

extern "C" __device__ u64 rz_dispatch_id() __asm__("llvm.amdgcn.dispatch.id");

__device__ __forceinline__ unsigned int pack_bf16x2(float lo, float hi) {
    const f32x2 v = {lo, hi};
    return __builtin_bit_cast(unsigned int, __builtin_convertvector(v, bf16x2));
}
__device__ __forceinline__ unsigned int rezero2(unsigned int x, unsigned int f, float a) {
    const unsigned int t = pack_bf16x2(a * __builtin_bit_cast(float, f << 16), a * __builtin_bit_cast(float, f & 0xffff0000u));
    return pack_bf16x2(__builtin_bit_cast(float, x << 16) + __builtin_bit_cast(float, t << 16),
                       __builtin_bit_cast(float, x & 0xffff0000u) + __builtin_bit_cast(float, t & 0xffff0000u));
}
__device__ __forceinline__ u32x4 rezero8(u32x4 x, u32x4 f, float a) {
    u32x4 o;
    o.x = rezero2(x.x, f.x, a); o.y = rezero2(x.y, f.y, a); o.z = rezero2(x.z, f.z, a); o.w = rezero2(x.w, f.w, a);
    return o;
}
__device__ __forceinline__ float load_alpha(const unsigned short* alpha) {
    const uintptr_t p = (uintptr_t)alpha;
    const unsigned int w = *(cu32p)(p & ~(uintptr_t)3);
    return __builtin_bit_cast(float, (p & 2) ? (w & 0xffff0000u) : (w << 16));
}
__device__ __forceinline__ unsigned int xcc_id() {
    unsigned int v;
    asm volatile("s_getreg_b32 %0, hwreg(HW_REG_XCC_ID, 0, 3)" : "=s"(v));
    return v;
}
__device__ __forceinline__ void lds_barrier() {
    asm volatile("s_waitcnt lgkmcnt(0)\n\ts_barrier" ::: "memory");
}
__device__ __forceinline__ unsigned int timed_load(const char* a) {
    u64 t0, t1;
    unsigned int w;
    asm volatile("s_memtime %0\n\ts_waitcnt lgkmcnt(0)\n\ts_load_dword %1, %3, 0x0\n\ts_waitcnt lgkmcnt(0)\n\ts_memtime %2\n\ts_waitcnt lgkmcnt(0)"
                 : "=&s"(t0), "=&s"(w), "=&s"(t1) : "s"(a) : "memory");
    return (unsigned int)(t1 - t0);
}
#define RZ_RSRC(p, n) __builtin_amdgcn_make_buffer_rsrc((void*)(p), 0, (n), 0x00020000)

__global__ __launch_bounds__(kBlock) void rezero_kernel(const void* __restrict__ x, const void* __restrict__ fx,
                                                        void* __restrict__ out, const unsigned short* __restrict__ alpha,
                                                        unsigned int nbytes, unsigned int nfixed, unsigned int qfixed,
                                                        unsigned int npairs, u64* __restrict__ ws) {
    const unsigned int b0 = blockIdx.x;
    const unsigned int b = b0 - kScouts;
    const unsigned int j = (b >> 3) * 4 + (b & 3);
    unsigned int t;
    if (__builtin_expect(b < nfixed, 1)) {
        if (((b0 >> 3) & 31) < 3) __builtin_amdgcn_s_sleep(kHoldCu);
        else if (xcc_id() < 2) __builtin_amdgcn_s_sleep(kHoldEarly);
        else __builtin_amdgcn_s_sleep(kHoldAll);
        t = 2 * (j + qfixed) + ((b >> 2) & 1);
    } else {
        const unsigned int lane = threadIdx.x & 63;
        const u64 did = rz_dispatch_id();
        const unsigned int tagH = (unsigned int)(did >> 8) ^ 0x5AC3E1F7u, tagL = ((unsigned int)did << 24) | 0x00A5D200u;
        if (__builtin_expect(b0 < kScouts, 0)) {
            __shared__ unsigned int s_lat[kLdsBytes / 4];
            const unsigned int w = b0 >> 3;
            if (w == 3 || nfixed == 0) return;
            const unsigned int wave = __builtin_amdgcn_readfirstlane(threadIdx.x >> 6);
            gu64* my = (gu64*)ws + (w * kRep + lane) * 8;
            const unsigned int pg = b0 & 1;
            const char* p = (const char*)(w == 0 ? x : (w == 1 ? fx : (const void*)out)) + (pg << 21) + ((wave ^ (wave >> 1)) & 1) * 8192;
            unsigned int h = (unsigned int)did * 0x9E3779B1u + (b0 * 4 + wave) * 0x85EBCA6Bu;
            u64 old = 0;
            if (wave == 0 && lane < kRep) old = __hip_atomic_load(my, __ATOMIC_RELAXED, __HIP_MEMORY_SCOPE_AGENT);
            char* pool = (char*)ws + (3 * kRep + w * kRounds) * 64;
            const unsigned int xcc = xcc_id(), tagS = tagH ^ 0x3C5A96E7u;
            const bool evenNearIf0 = ((xcc >> 2) ^ pg) == 0;
            unsigned int lat = ~0u;
#pragma unroll
            for (int k = 0; k < kRounds; ++k) {
                const unsigned int l = timed_load(p + ((h >> 20) & 63) * 16384 + ((h >> 26) & 31) * 256);
                h = h * 0x2C1B3C6Du + 0x297A2D39u;
                lat = (l >= 380) ? min(lat, l) : lat;
                if (lane == 0) s_lat[wave] = lat;
                lds_barrier();
                const unsigned int l0 = __builtin_amdgcn_readfirstlane(s_lat[0]), l1 = __builtin_amdgcn_readfirstlane(s_lat[1]);
                const unsigned int l2 = __builtin_amdgcn_readfirstlane(s_lat[2]), l3 = __builtin_amdgcn_readfirstlane(s_lat[3]);
                const unsigned int la = min(min(l0, l3), 0xFFFFu), lb = min(min(l1, l2), 0xFFFFu);
                unsigned int u = evenNearIf0 ? la : lb, v = evenNearIf0 ? lb : la;
                if (threadIdx.x == 64)
                    __hip_atomic_store((gu64*)(uintptr_t)(pool + k * 64) + (b0 & 7), ((u64)tagS << 32) | (u << 16) | v, __ATOMIC_RELAXED,
                                       __HIP_MEMORY_SCOPE_AGENT);
                bool sure = min(u, v) < 800 && max(u, v) - min(u, v) >= 80 && max(u, v) != 0xFFFFu;
                if (k == 0) {
                    sure = sure || (max(u, v) - min(u, v) >= 80 && min(u, v) < 1500 && max(max(l0, l1), max(l2, l3)) != ~0u &&
                                    (l0 < l1) == (l3 < l2) && (l0 < l1) == (la < lb));
                } else if (!sure) {
                    const u32x16 o = *(cu32x16p)(uintptr_t)(pool + (k - 1) * 64);
                    unsigned int samples = 2 * k;
#pragma unroll
                    for (int i = 0; i < 8; ++i) {
                        const bool fresh = o[2 * i + 1] == tagS && i != (int)(b0 & 7);
                        u = fresh ? min(u, o[2 * i] >> 16) : u;
                        v = fresh ? min(v, o[2 * i] & 0xFFFFu) : v;
                        samples += (fresh && k > 1) ? 2 : 0;
                    }
                    sure = max(u, v) - min(u, v) >= (samples >= 6 ? 60u : 120u) && min(u, v) < 1100 && max(u, v) != 0xFFFFu;
                }
                if (sure || k == kRounds - 1) {
                    bool answered = false;
                    if (xcc >= 2 || k > 0) {
                        const u64 z = *(cu64p)(uintptr_t)((const char*)ws + (w * kRep + (b0 & 7) * 4 + k) * 64);
                        answered = (unsigned int)(z >> 32) == tagH && ((unsigned int)z ^ tagL) < 256;
                    }
                    u64 expected = old;
                    asm volatile("" : "+v"(expected));
                    const bool set = ((unsigned int)(expected >> 32) == tagH) && (((unsigned int)expected ^ tagL) < 256);
                    if (wave == 0 && lane < kRep && !set && !answered) {
                        const u64 want = ((u64)tagH << 32) | tagL | (((xcc - b0) & 7) << 1) | (u <= v ? 0u : 1u);
                        __hip_atomic_compare_exchange_strong(my, &expected, want, __ATOMIC_RELAXED, __ATOMIC_RELAXED, __HIP_MEMORY_SCOPE_AGENT);
                    }
                    break;
                }
                lds_barrier();
            }
            return;
        }
        if (__builtin_expect(j >= npairs || nfixed == 0, 0)) {
            const unsigned int npad = (npairs + 3) / 4 * 8;
            if (j < npairs && b < npad) {
                t = 2 * j + ((b >> 2) & 1);
            } else {
                if (b < npad) return;
                t = 2 * npairs + (b - npad);
                if ((nbytes & 2) && b == npad && threadIdx.x == 0) {
                    const unsigned int i = nbytes / 2 - 1;
                    const unsigned int v = rezero2(((const unsigned short*)x)[i], ((const unsigned short*)fx)[i], load_alpha(alpha));
                    ((unsigned short*)out)[i] = (unsigned short)v;
                }
            }
        } else {
            const unsigned int q = j - nfixed / 2;
            const char* wl = (const char*)ws + (((b >> 3) + (b >> 8)) & (kRep - 1)) * 64;
            const u64 v0 = *(cu64p)(uintptr_t)wl, v1 = *(cu64p)(uintptr_t)(wl + kRep * 64), v2 = *(cu64p)(uintptr_t)(wl + 2 * kRep * 64);
            unsigned int d0 = (unsigned int)v0, d1 = (unsigned int)v1, d2 = (unsigned int)v2;
            if (__builtin_expect(!((unsigned int)(v0 >> 32) == tagH && (unsigned int)(v1 >> 32) == tagH && (unsigned int)(v2 >> 32) == tagH &&
                                   ((d0 ^ tagL) | (d1 ^ tagL) | (d2 ^ tagL)) < 256), 0)) {
                gu64* my = (gu64*)(uintptr_t)(wl + (lane < 3 ? lane : 0) * kRep * 64);
                for (unsigned int spins = 0;; ++spins) {
                    u64 v = __hip_atomic_load(my, __ATOMIC_RELAXED, __HIP_MEMORY_SCOPE_AGENT);
                    const bool set = ((unsigned int)(v >> 32) == tagH) && (((unsigned int)v ^ tagL) < 256);
                    if (__all(set)) {
                        d0 = __builtin_amdgcn_readlane((unsigned int)v, 0);
                        d1 = __builtin_amdgcn_readlane((unsigned int)v, 1);
                        d2 = __builtin_amdgcn_readlane((unsigned int)v, 2);
                        break;
                    }
                    if (spins >= 32 && lane < 3 && !set)
                        __hip_atomic_compare_exchange_strong(my, &v, ((u64)tagH << 32) | tagL, __ATOMIC_RELAXED, __ATOMIC_RELAXED,
                                                             __HIP_MEMORY_SCOPE_AGENT);
                    __builtin_amdgcn_s_sleep(4);
                }
            }
            const unsigned int m = (d0 & d1) | (d0 & d2) | (d1 & d2);
            t = 2 * q + ((((b + (d0 >> 1)) >> 2) ^ m ^ (q >> 7)) & 1);
        }
    }

    const __amdgpu_buffer_rsrc_t rx = RZ_RSRC(x, nbytes), rf = RZ_RSRC(fx, nbytes), ro = RZ_RSRC(out, nbytes);
    const unsigned int off = t * 8192 + threadIdx.x * 16;
    const u32x4 x0 = __builtin_amdgcn_raw_buffer_load_b128(rx, off, 0, kLoadAux);
    const u32x4 f0 = __builtin_amdgcn_raw_buffer_load_b128(rf, off, 0, kLoadAux);
    const u32x4 x1 = __builtin_amdgcn_raw_buffer_load_b128(rx, off + 4096, 0, kLoadAux);
    const u32x4 f1 = __builtin_amdgcn_raw_buffer_load_b128(rf, off + 4096, 0, kLoadAux);
    __builtin_amdgcn_sched_barrier(0);
    const float a = load_alpha(alpha);
    __builtin_amdgcn_raw_buffer_store_b128(rezero8(x0, f0, a), ro, off, 0, kStoreAux);
    __builtin_amdgcn_raw_buffer_store_b128(rezero8(x1, f1, a), ro, off + 4096, 0, kStoreAux);
}

extern "C" void kernel_launch(void* const* d_in, const int* in_sizes, int n_in,
                              void* d_out, int out_size, void* d_ws, size_t ws_size,
                              hipStream_t stream) {
    const unsigned int nbytes = (unsigned int)out_size * 2u;
    const unsigned int npairs = nbytes >> 14;
    const unsigned int npad = (npairs + 3) / 4 * 8;
    const unsigned int nrem = ((nbytes & 16383u) + 8191u) / 8192u;
    const bool big = npairs >= kFixed / 2 + 256;
    const unsigned int nfixed = big ? kFixed : 0u, qfixed = big ? npairs - kFixed / 2 : 0u;
    rezero_kernel<<<kScouts + npad + nrem, kBlock, 0, stream>>>(d_in[0], d_in[1], d_out, (const unsigned short*)d_in[2], nbytes,
                                                                nfixed, qfixed, npairs, (u64*)d_ws);
}
```
